```python
import torch

def _softmax(x, axis=-1):
    return torch.softmax(x, dim=axis)
def _gelu(x):
    return torch.nn.functional.gelu(x, approximate="tanh")
def _silu(x):
    return torch.nn.functional.silu(x)
def _sigmoid(x):
    return torch.sigmoid(x)
def _layernorm(x, axis=-1, eps=1e-5):
    m = x.mean(dim=axis, keepdim=True)
    v = x.var(dim=axis, keepdim=True, unbiased=False)
    return (x - m) / torch.sqrt(v + eps)
def _scatter_add(tgt, idx, src, axis=0):
    return tgt.clone().index_add_(axis, idx, src)
def _segment_sum(src, seg, n):
    out = torch.zeros((n,) + src.shape[1:], dtype=src.dtype)
    return out.index_add_(0, seg, src)
def _pad(x, pw):
    flat = tuple(v for pair in reversed(pw) for v in pair)
    return torch.nn.functional.pad(x, flat)

kernel_name = 'recurrent_scan__sliding_local_dilated__glu_gated_glu__prompt'

def setup_inputs():

    _w = (lambda a: (lambda t: t.to(torch.bfloat16) if t.is_floating_point() else t)(torch.as_tensor(a)))
    def _A_setup():
        B, S, D = 1, 2048, 512
        return {
            "x": _w((lambda *s: torch.randn(*s, dtype=torch.bfloat16) * 0.05)(B, S, D)),
            "wqkv": _w((lambda *s: torch.randn(*s, dtype=torch.bfloat16) * 0.05)(D, 3 * D)),
            "wo": _w((lambda *s: torch.randn(*s, dtype=torch.bfloat16) * 0.05)(D, D)),
        }
    _A_in = _A_setup()

    def _B_setup():
        B, S, D, F = 1, 2048, 512, 1792
        return {
            "x": _w((lambda *s: torch.randn(*s, dtype=torch.bfloat16) * 0.05)(B, S, D)),
            "w_gate": _w((lambda *s: torch.randn(*s, dtype=torch.bfloat16) * 0.05)(D, F)),
            "w_up": _w((lambda *s: torch.randn(*s, dtype=torch.bfloat16) * 0.05)(D, F)),
            "w_down": _w((lambda *s: torch.randn(*s, dtype=torch.bfloat16) * 0.05)(F, D)),
        }
    _B_in = _B_setup()

    return {
        "A_x": _A_in["x"],
        "A_wqkv": _A_in["wqkv"],
        "A_wo": _A_in["wo"],
        "B_x": _B_in["x"],
        "B_w_gate": _B_in["w_gate"],
        "B_w_up": _B_in["w_up"],
        "B_w_down": _B_in["w_down"],
    }

def reference(A_x, A_wqkv, A_wo, B_x, B_w_gate, B_w_up, B_w_down):
    def _A_ref(x, wqkv, wo):
        B, S, D = x.shape
        H = 32; Dh = D // H
        W, dil = 8, 2
        qkv = (lambda x, *d: x.permute(*d))((x @ wqkv).reshape(B, S, 3, H, Dh), 2, 0, 3, 1, 4)
        q, k, v = qkv[0], qkv[1], qkv[2]
        scale = Dh ** -0.5
        pos = torch.arange(S)
        off = torch.arange(W) * dil
        idx = pos[:, None] - off[None, :]
        mask = idx >= 0
        idx_c = torch.where(mask, idx, 0).reshape(-1)
        kg = k[:, :, idx_c, :].reshape(B, H, S, W, Dh)
        vg = v[:, :, idx_c, :].reshape(B, H, S, W, Dh)
        logits = torch.einsum("bhsd,bhswd->bhsw", q, kg) * scale
        logits = torch.where(mask[None, None, :, :], logits, -1e9)
        attn = _softmax(logits, axis=-1)
        o = torch.einsum("bhsw,bhswd->bhsd", attn, vg)
        return (lambda x, *d: x.permute(*d))(o, 0, 2, 1, 3).reshape(B, S, D) @ wo

    def _B_ref(x, w_gate, w_up, w_down):
        g = torch.einsum("bsd,df->bsf", x, w_gate)
        u = torch.einsum("bsd,df->bsf", x, w_up)
        h = _sigmoid(g) * u
        return torch.einsum("bsf,fd->bsd", h, w_down)

    x = A_x
    xn = _layernorm(x, axis=-1)
    s = _A_ref(xn, A_wqkv, A_wo)
    g = _silu(_B_ref(xn, B_w_gate, B_w_up, B_w_down))
    return x + s * g
```

```cpp
#include <hip/hip_runtime.h>
#include <hip/hip_bf16.h>
typedef __hip_bfloat16 bf16;
static __device__ __forceinline__ float b2f(bf16 v){ return __bfloat162float(v);}
static __device__ __forceinline__ bf16 f2b(float v){ return __float2bfloat16(v);}
static __device__ __forceinline__ float rb(float v){ return b2f(f2b(v)); }

__global__ void ln_k(const bf16* x, bf16* xn){
  int row = blockIdx.x; int t = threadIdx.x;
  float v[8]; float s=0;
  for(int j=0;j<8;j++){ v[j]=b2f(x[row*512+t*8+j]); s+=v[j]; }
  for(int o=32;o>0;o>>=1) s+=__shfl_xor(s,o);
  float m=s/512.f; float q=0;
  for(int j=0;j<8;j++){ q+=(v[j]-m)*(v[j]-m);}
  for(int o=32;o>0;o>>=1) q+=__shfl_xor(q,o);
  float var=rb(q/512.f); float mb=rb(m);
  float sd=rb(sqrtf(rb(var+1e-5f)));
  for(int j=0;j<8;j++) xn[row*512+t*8+j]=f2b(rb(v[j]-mb)/sd);
}
__global__ void gemm_k(const bf16* A, const bf16* B, bf16* C, int M,int N,int K){
  __shared__ float As[16][17], Bs[16][17];
  int tx=threadIdx.x, ty=threadIdx.y; int r=blockIdx.y*16+ty, c=blockIdx.x*16+tx; float acc=0;
  for(int k0=0;k0<K;k0+=16){ As[ty][tx]=b2f(A[r*K+k0+tx]); Bs[ty][tx]=b2f(B[(k0+ty)*N+c]); __syncthreads();
    for(int k=0;k<16;k++) acc+=As[ty][k]*Bs[k][tx]; __syncthreads(); }
  C[r*N+c]=f2b(acc);
}
__global__ void attn_k(const bf16* qkv, bf16* o){
  int idx=blockIdx.x*blockDim.x+threadIdx.x; int s=idx/32, h=idx%32;
  float q[16]; for(int d=0;d<16;d++) q[d]=b2f(qkv[s*1536+h*16+d]);
  float l[8]; float mx=-1e30f;
  for(int w=0;w<8;w++){ int p=s-2*w; if(p<0){l[w]=-1e9f;} else { float a=0; for(int d=0;d<16;d++) a+=q[d]*b2f(qkv[p*1536+512+h*16+d]); l[w]=rb(rb(a)*0.25f);} mx=fmaxf(mx,l[w]); }
  float sum=0; for(int w=0;w<8;w++){ l[w]=__expf(l[w]-mx); sum+=l[w]; }
  for(int d=0;d<16;d++){ float a=0; for(int w=0;w<8;w++){ int p=s-2*w; if(p<0)p=0; a+=rb(l[w]/sum)*b2f(qkv[p*1536+1024+h*16+d]); } o[s*512+h*16+d]=f2b(a);}
}
__global__ void glu_k(const bf16* g,const bf16* u, bf16* h,int n){ int i=blockIdx.x*blockDim.x+threadIdx.x; if(i<n){ float gg=b2f(g[i]); float sg=rb(1.f/(1.f+__expf(-gg))); h[i]=f2b(sg*b2f(u[i])); } }
__global__ void fin_k(const bf16* x,const bf16* s,const bf16* d, bf16* out,int n){ int i=blockIdx.x*blockDim.x+threadIdx.x; if(i<n){ float dd=b2f(d[i]); float g=rb(dd/(1.f+__expf(-dd))); out[i]=f2b(b2f(x[i])+rb(b2f(s[i])*g)); } }

extern "C" void kernel_launch(void* const* d_in, const int* in_sizes, int n_in, void* d_out, int out_size, void* d_ws, size_t ws_size, hipStream_t stream){
  const bf16* x=(const bf16*)d_in[0]; const bf16* wqkv=(const bf16*)d_in[1]; const bf16* wo=(const bf16*)d_in[2];
  const bf16* wg=(const bf16*)d_in[4]; const bf16* wu=(const bf16*)d_in[5]; const bf16* wd=(const bf16*)d_in[6];
  bf16* ws=(bf16*)d_ws; bf16* xn=ws; bf16* qkv=xn+2048*512; bf16* o=qkv+2048*1536; bf16* g=o+2048*512; bf16* u=g+2048*1792; bf16* h=u+2048*1792; bf16* s=h+2048*1792; bf16* d=s+2048*512;
  ln_k<<<2048,64,0,stream>>>(x,xn);
  dim3 b(16,16);
  gemm_k<<<dim3(1536/16,128),b,0,stream>>>(xn,wqkv,qkv,2048,1536,512);
  gemm_k<<<dim3(1792/16,128),b,0,stream>>>(xn,wg,g,2048,1792,512);
  gemm_k<<<dim3(1792/16,128),b,0,stream>>>(xn,wu,u,2048,1792,512);
  attn_k<<<2048*32/256,256,0,stream>>>(qkv,o);
  glu_k<<<2048*1792/256,256,0,stream>>>(g,u,h,2048*1792);
  gemm_k<<<dim3(512/16,128),b,0,stream>>>(o,wo,s,2048,512,512);
  gemm_k<<<dim3(512/16,128),b,0,stream>>>(h,wd,d,2048,512,1792);
  fin_k<<<2048*512/256,256,0,stream>>>(x,s,d,(bf16*)d_out,2048*512);
}
```

```cpp
#include <hip/hip_runtime.h>
#include <hip/hip_bf16.h>

typedef short s16x4 __attribute__((ext_vector_type(4)));
typedef short s16x8 __attribute__((ext_vector_type(8)));
typedef __bf16 bf2 __attribute__((ext_vector_type(2)));
typedef float f32x2 __attribute__((ext_vector_type(2)));
typedef float f32x4 __attribute__((ext_vector_type(4)));
typedef int i32x4 __attribute__((ext_vector_type(4)));
typedef unsigned u32x2 __attribute__((ext_vector_type(2)));
typedef unsigned u32x4 __attribute__((ext_vector_type(4)));
typedef unsigned long long u64;
#define LDS __attribute__((address_space(3)))
#define GLB __attribute__((address_space(1)))
#define DEV __device__ __forceinline__
#define SB() __builtin_amdgcn_sched_barrier(0)

#define ROTG 1

constexpr int L_W = 0;
constexpr int L_X = 65536;
constexpr int XSLOT = 24576;
constexpr int L_HALO = 139264;
constexpr int L_MISC = 155648;
constexpr int L_STAT = L_MISC;
constexpr int L_CS = L_MISC + 2560;
constexpr int L_TAG = L_MISC + 3584;
constexpr int L_SYNC = L_MISC + 4096;
constexpr int LDS_BYTES = 163840;

constexpr size_t WS_A = 0;
constexpr size_t WS_A_BYTES = 32u * 72u * 4096u;
constexpr size_t WS_FLAG = WS_A_BYTES;
constexpr size_t WS_EPOCH = WS_FLAG + 256 * 512;
constexpr size_t WS_STAMP = 32u << 20;

#define IS_MLP_R(r) ((r) < 21)
struct Params {
  const char* x; const char* wqkv; const char* wo; const char* wg; const char* wu; const char* wd;
  char* out; char* ws;
};

DEV i32x4 make_srd(const void* p, unsigned bytes) {
  u64 a = (u64)p;
  i32x4 r; r[0] = (int)(unsigned)a; r[1] = (int)((unsigned)(a >> 32) & 0xffffu); r[2] = (int)bytes; r[3] = 0x00020000;
  return r;
}
DEV void dma16(unsigned lds, unsigned voff, i32x4 srd, unsigned soff) {
  asm volatile("s_mov_b32 m0, %0\n\ts_nop 0\n\tbuffer_load_dwordx4 %1, %2, %3 offen lds"
               :: "s"(lds), "v"(voff), "s"(srd), "s"(soff) : "memory");
}
DEV void dma16m(unsigned lds, unsigned voff, i32x4 srd, unsigned soff, u64 mask) {
  asm volatile("s_mov_b32 m0, %0\n\ts_mov_b64 exec, %4\n\tbuffer_load_dwordx4 %1, %2, %3 offen lds\n\ts_mov_b64 exec, -1"
               :: "s"(lds), "v"(voff), "s"(srd), "s"(soff), "s"(mask) : "memory");
}
DEV void store16_sc1(i32x4 v, unsigned voff, i32x4 srd, unsigned soff) {
  asm volatile("buffer_store_dwordx4 %0, %1, %2, %3 offen sc1\n\ts_nop 1" :: "v"(v), "v"(voff), "s"(srd), "s"(soff) : "memory");
}
template <int N> DEV void waitv() { asm volatile("s_waitcnt vmcnt(%0)" :: "n"(N) : "memory"); }
DEV void waitl() { __builtin_amdgcn_s_waitcnt(0xC07F); asm volatile("" ::: "memory"); }
DEV void bar() { asm volatile("s_barrier" ::: "memory"); }
DEV s16x8 lds_b128(unsigned a) { return *(const LDS s16x8*)(size_t)a; }
DEV s16x4 lds_tr4(unsigned a) { return __builtin_amdgcn_ds_read_tr16_b64_v4i16((LDS s16x4*)(size_t)a); }
DEV s16x8 cat(s16x4 lo, s16x4 hi) { return __builtin_shufflevector(lo, hi, 0, 1, 2, 3, 4, 5, 6, 7); }
DEV s16x8 lds_tr(unsigned a0, unsigned a1) { return cat(lds_tr4(a0), lds_tr4(a1)); }
DEV f32x4 ldsf4(unsigned a) { return *(const LDS f32x4*)(size_t)a; }
DEV float ldsf(unsigned a) { return *(const LDS float*)(size_t)a; }
DEV f32x4 mfma32(s16x8 a, s16x8 b, f32x4 c) { return __builtin_amdgcn_mfma_f32_16x16x32_bf16(a, b, c, 0, 0, 0); }
DEV f32x4 mfma16(u32x2 a, u32x2 b, f32x4 c) {
  return __builtin_amdgcn_mfma_f32_16x16x16bf16_1k(__builtin_bit_cast(s16x4, a), __builtin_bit_cast(s16x4, b), c, 0, 0, 0);
}
DEV float dot2(unsigned a, unsigned b, float c) {
  return __builtin_amdgcn_fdot2_f32_bf16(__builtin_bit_cast(bf2, a), __builtin_bit_cast(bf2, b), c, false);
}
DEV unsigned pack2(float a, float b) { bf2 p = {(__bf16)a, (__bf16)b}; return __builtin_bit_cast(unsigned, p); }
DEV unsigned pack2(f32x2 a) { return pack2(a[0], a[1]); }
DEV float rbf(float a) { return (float)(__bf16)a; }
DEV f32x2 lo2(f32x4 a) { return f32x2{a[0], a[1]}; }
DEV f32x2 hi2(f32x4 a) { return f32x2{a[2], a[3]}; }
DEV f32x2 fma2(f32x2 a, f32x2 b, f32x2 c) { return __builtin_elementwise_fma(a, b, c); }
DEV f32x2 sp2(float a) { return f32x2{a, a}; }
DEV float red4(float v0, float v1, float v2, float v3) {
  auto a = __builtin_amdgcn_permlane32_swap(__float_as_uint(v0), __float_as_uint(v2), false, false);
  auto b = __builtin_amdgcn_permlane32_swap(__float_as_uint(v1), __float_as_uint(v3), false, false);
  float s02 = __uint_as_float(a[0]) + __uint_as_float(a[1]);
  float s13 = __uint_as_float(b[0]) + __uint_as_float(b[1]);
  auto c = __builtin_amdgcn_permlane16_swap(__float_as_uint(s02), __float_as_uint(s13), false, false);
  return __uint_as_float(c[0]) + __uint_as_float(c[1]);
}
DEV float allq_max(float v) {
  auto a = __builtin_amdgcn_permlane32_swap(__float_as_uint(v), __float_as_uint(v), false, false);
  v = fmaxf(__uint_as_float(a[0]), __uint_as_float(a[1]));
  auto b = __builtin_amdgcn_permlane16_swap(__float_as_uint(v), __float_as_uint(v), false, false);
  return fmaxf(__uint_as_float(b[0]), __uint_as_float(b[1]));
}
DEV int fresh_lane() { int l = __builtin_amdgcn_mbcnt_hi(~0u, __builtin_amdgcn_mbcnt_lo(~0u, 0u)); asm volatile("" : "+v"(l)); return l; }
DEV int tok_of(int rb, int i) { return 32 * (rb >> 1) + 2 * i + (rb & 1); }
DEV void lds_spin4(unsigned a) {
  for (;;) {
    u32x4 v = *(volatile LDS u32x4*)(size_t)a;
    if (__builtin_amdgcn_readfirstlane(v[0] & v[1] & v[2] & v[3])) break;
    __builtin_amdgcn_s_sleep(1);
  }
  asm volatile("" ::: "memory");
}
DEV void poll_flags(GLB u64* fl, bool need, u64 tag) {
  unsigned spins = 0;
  for (;;) {
    u64 v = __hip_atomic_load(fl, __ATOMIC_RELAXED, __HIP_MEMORY_SCOPE_AGENT);
    if (__all(!need || v == tag)) break;
    if (++spins > (1u << 20)) break;
    __builtin_amdgcn_s_sleep(1);
  }
  __builtin_amdgcn_fence(__ATOMIC_ACQUIRE, "wavefront");
  asm volatile("" ::: "memory");
}

#define STAMP(n)
#define TSARG
#define TSPASS

constexpr float LOG2E = 1.4426950408889634f;

template <bool QKV, int NB, int NC>
struct S1 { f32x4 acc[NB][NC]; float cs[NC]; };

template <bool QKV> DEV s16x4 w_half(int idx, unsigned wa0, unsigned wa1) {
  const int c = idx >> 1, t = idx & 1;
  if (QKV) return lds_tr4(wa0 + c * 128 + t * 4 * 384);
  return lds_tr4(((c & 1) ? wa1 : wa0) + (c >> 1) * 8192 + t * 1024);
}
template <bool QKV> DEV s16x8 x_frag(int b, unsigned xa, unsigned ha) {
  if (QKV) return b == 0 ? lds_b128(ha) : lds_b128(xa + (b - 1) * 2048);
  return lds_b128(xa + b * 2048);
}
constexpr int PF = 3;
template <bool QKV, int NB, int NC, bool LAST>
DEV void c_step(S1<QKV, NB, NC>& st, s16x8 (&wf)[NC], s16x8 (&xp)[PF],
                unsigned xa, unsigned ha, unsigned xan, unsigned han, unsigned wn0, unsigned wn1) {
  s16x8 xf[NB + PF]; s16x4 wh[2 * NC];
#pragma unroll
  for (int j = 0; j < PF; j++) xf[j] = xp[j];
#pragma unroll
  for (int b = 0; b < NB; b++) {
    if (!LAST && b < NC) { wh[2 * b] = w_half<QKV>(2 * b, wn0, wn1); wh[2 * b + 1] = w_half<QKV>(2 * b + 1, wn0, wn1); }
    if (b + PF < NB) xf[b + PF] = x_frag<QKV>(b + PF, xa, ha);
    else if (!LAST) xf[b + PF] = x_frag<QKV>(b + PF - NB, xan, han);
#pragma unroll
    for (int c = 0; c < NC; c++) {
      if (QKV && b == 0 && c == 0) continue;
      if (QKV && c == 2) st.acc[b][c] = mfma32(xf[b], wf[c], st.acc[b][c]);
      else st.acc[b][c] = mfma32(wf[c], xf[b], st.acc[b][c]);
    }
    if (b < 2 * NC) {
      u32x4 u = __builtin_bit_cast(u32x4, wf[b >> 1]);
      st.cs[b >> 1] = dot2(u[2 * (b & 1)], 0x3f803f80u, st.cs[b >> 1]);
      st.cs[b >> 1] = dot2(u[2 * (b & 1) + 1], 0x3f803f80u, st.cs[b >> 1]);
    }
    SB();
  }
  if (!LAST) {
#pragma unroll
    for (int c = 0; c < NC; c++) wf[c] = cat(wh[2 * c], wh[2 * c + 1]);
#pragma unroll
    for (int j = 0; j < PF; j++) xp[j] = xf[NB + j];
  }
}
template <bool QKV, int NB, int NC>
DEV void c_main(S1<QKV, NB, NC>& st, unsigned xa, unsigned wa0, unsigned wa1 TSARG) {
#pragma unroll
  for (int b = 0; b < NB; b++)
#pragma unroll
    for (int c = 0; c < NC; c++) st.acc[b][c] = f32x4{0.f, 0.f, 0.f, 0.f};
#pragma unroll
  for (int c = 0; c < NC; c++) st.cs[c] = 0.f;
  s16x8 wf[NC], xp[PF];
  bar();
  STAMP(1);
#pragma unroll
  for (int c = 0; c < NC; c++) wf[c] = cat(w_half<QKV>(2 * c, L_W + wa0, L_W + wa1), w_half<QKV>(2 * c + 1, L_W + wa0, L_W + wa1));
#pragma unroll
  for (int j = 0; j < PF; j++) xp[j] = x_frag<QKV>(j, L_X + xa, L_HALO + xa);
  waitl();
  unsigned xb = L_X, hb = L_HALO, wb = 0;
#pragma unroll 1
  for (int it = 0; it < 8; it++) {
    unsigned xbn = (xb == L_X + 2 * XSLOT) ? L_X : xb + XSLOT;
    bar();
    c_step<QKV, NB, NC, false>(st, wf, xp, xb + xa, hb + xa, xb + (xa ^ 64), hb + (xa ^ 64), wb + 16384 + wa0, wb + 16384 + wa1);
    wb ^= 32768;
    bar();
    c_step<QKV, NB, NC, false>(st, wf, xp, xb + (xa ^ 64), hb + (xa ^ 64), xbn + xa, hb + 2048 + xa, wb + wa0, wb + wa1);
    xb = xbn; hb += 2048;
  }
}

template <int NB>
DEV void mlp_path(const Params& p, int w, int rb0, int S TSARG) {
  int lane = fresh_lane(), i = lane & 15, q = lane >> 4;
  S1<false, NB, 4> st;
  const unsigned xa = i * 128 + ((q ^ (i >> 1)) & 7) * 16;
  const int krow = 8 * q + (i >> 2);
  const int f = (krow & 3) | (((krow >> 3) & 1) << 2);
  const unsigned wa0 = krow * 256 + (((2 * w) ^ f) & 7) * 32 + (i & 3) * 8;
  c_main<false, NB, 4>(st, xa, wa0, wa0 ^ 32 TSPASS);
  lane = fresh_lane(); i = lane & 15; q = lane >> 4;
  *(LDS float*)(size_t)(L_CS + w * 256 + lane * 4) = red4(st.cs[0], st.cs[1], st.cs[2], st.cs[3]);
  bar();
  STAMP(2);
  f32x4 cs[4];
#pragma unroll
  for (int c = 0; c < 4; c++) cs[c] = ldsf4(L_CS + w * 256 + c * 64 + q * 16);
  float nm[NB + 1], sd[NB + 1], c1[NB + 1];
  nm[0] = ldsf(L_STAT + i * 4); sd[0] = ldsf(L_STAT + 64 + i * 4); c1[0] = ldsf(L_STAT + 128 + i * 4);
  const i32x4 sa = make_srd(p.ws + WS_A, (unsigned)WS_A_BYTES);
  const int ks = 16 + 4 * S + w;
#pragma unroll
  for (int b = 0; b < NB; b++) {
    f32x2 z[4], u[4];
    if (b + 1 < NB) { nm[b + 1] = ldsf(L_STAT + (b + 1) * 192 + i * 4); sd[b + 1] = ldsf(L_STAT + (b + 1) * 192 + 64 + i * 4); c1[b + 1] = ldsf(L_STAT + (b + 1) * 192 + 128 + i * 4); }
#pragma unroll
    for (int j = 0; j < 4; j++) {
      f32x2 a = (j & 1) ? hi2(st.acc[b][j >> 1]) : lo2(st.acc[b][j >> 1]);
      f32x2 c = (j & 1) ? hi2(cs[j >> 1]) : lo2(cs[j >> 1]);
      z[j] = fma2(c, sp2(nm[b]), a);
    }
    SB();
#pragma unroll
    for (int j = 0; j < 4; j++) z[j] = z[j] * sp2(c1[b]);
    SB();
#pragma unroll
    for (int j = 0; j < 4; j++) {
      z[j][0] = __builtin_amdgcn_exp2f(z[j][0]); z[j][1] = __builtin_amdgcn_exp2f(z[j][1]);
      f32x2 a = (j & 1) ? hi2(st.acc[b][2 + (j >> 1)]) : lo2(st.acc[b][2 + (j >> 1)]);
      f32x2 c = (j & 1) ? hi2(cs[2 + (j >> 1)]) : lo2(cs[2 + (j >> 1)]);
      u[j] = fma2(c, sp2(nm[b]), a);
    }
    SB();
#pragma unroll
    for (int j = 0; j < 4; j++) z[j] = fma2(z[j], sp2(sd[b]), sp2(sd[b]));
    SB();
#pragma unroll
    for (int j = 0; j < 4; j++) { z[j][0] = __builtin_amdgcn_rcpf(z[j][0]); z[j][1] = __builtin_amdgcn_rcpf(z[j][1]); }
    SB();
#pragma unroll
    for (int j = 0; j < 4; j++) u[j] = u[j] * z[j];
    SB();
    i32x4 v = {(int)pack2(u[0]), (int)pack2(u[1]), (int)pack2(u[2]), (int)pack2(u[3])};
    const int rb = rb0 + b;
    store16_sc1(v, lane * 16, sa, (((rb >> 2) * 72 + ks) * 4 + (rb & 3)) * 1024);
  }
}

DEV void qkv_path(const Params& p, int w, int P, int G TSARG) {
  int lane = fresh_lane(), i = lane & 15, q = lane >> 4;
  S1<true, 13, 3> st;
  const unsigned xa = i * 128 + ((q ^ (i >> 1)) & 7) * 16;
  const int krow = 8 * q + (i >> 2);
  const int f = ((krow >> 1) & 1) | (((krow >> 3) & 1) << 1);
  const unsigned wa0 = krow * 384 + ((w ^ f) & 3) * 32 + (i & 3) * 8;
  c_main<true, 13, 3>(st, xa, wa0, wa0 TSPASS);
  lane = fresh_lane(); i = lane & 15; q = lane >> 4;
  *(LDS float*)(size_t)(L_CS + w * 256 + lane * 4) = red4(st.cs[0], st.cs[1], st.cs[2], st.cs[2]);
  bar();
  STAMP(2);
  const f32x4 csq = ldsf4(L_CS + w * 256 + q * 16), csk = ldsf4(L_CS + w * 256 + 64 + q * 16);
  const float ncsv = ldsf(L_CS + w * 256 + 128 + i * 4);
  u32x2 Q[13], K[13], V[13]; float qs[13];
  auto fold = [&](int b) {
    const int tb = L_STAT + (b == 0 ? 12 : b - 1) * 192;
    const float nm = ldsf(tb + i * 4), rstd = ldsf(tb + 128 + i * 4);
    const f32x4 m4 = ldsf4(tb + q * 16), r4 = ldsf4(tb + 128 + q * 16);
    if (b > 0) {
      Q[b] = u32x2{pack2(fma2(lo2(csq), sp2(nm), lo2(st.acc[b][0]))), pack2(fma2(hi2(csq), sp2(nm), hi2(st.acc[b][0])))};
      qs[b] = rstd * (0.25f * LOG2E);
    }
    K[b] = u32x2{pack2(fma2(lo2(csk), sp2(nm), lo2(st.acc[b][1])) * sp2(rstd)), pack2(fma2(hi2(csk), sp2(nm), hi2(st.acc[b][1])) * sp2(rstd))};
    V[b] = u32x2{pack2(fma2(lo2(m4), sp2(ncsv), lo2(st.acc[b][2])) * lo2(r4)), pack2(fma2(hi2(m4), sp2(ncsv), hi2(st.acc[b][2])) * hi2(r4))};
  };
  fold(0);
  const float NEG = -__builtin_inff();
  f32x4 mcur, mprev, mh0, mh1; u32x2 selc;
  const bool first = (P == 0);
#pragma unroll
  for (int r = 0; r < 4; r++) {
    int kap = 4 * q + r;
    mcur[r] = (kap <= i && kap >= i - 7) ? 0.f : NEG;
    mprev[r] = (kap >= i + 9) ? 0.f : NEG;
    mh0[r] = (!first && kap < 8 && kap >= i + 1) ? 0.f : NEG;
    mh1[r] = (!first && kap >= i + 9) ? 0.f : NEG;
  }
  selc[0] = ((4 * q <= i) ? 0xffffu : 0u) | ((4 * q + 1 <= i) ? 0xffff0000u : 0u);
  selc[1] = ((4 * q + 2 <= i) ? 0xffffu : 0u) | ((4 * q + 3 <= i) ? 0xffff0000u : 0u);
  const i32x4 sa = make_srd(p.ws + WS_A, (unsigned)WS_A_BYTES);
  const int head = 4 * G + w;
  const unsigned voff = (q & 1) * 1024 + ((2 * (head & 1) + (q >> 1)) * 16 + i) * 16;
  const u32x2 ones = {0x3f803f80u, 0x3f803f80u};
  const f32x4 zero = {0.f, 0.f, 0.f, 0.f};
#pragma unroll
  for (int g2 = 0; g2 < 3; g2++) {
    f32x4 sc[4], sp[4]; float mx[4]; u32x2 pk[4]; f32x4 o[4], sm[4];
#pragma unroll
    for (int u = 0; u < 4; u++) fold(1 + 4 * g2 + u);
#pragma unroll
    for (int u = 0; u < 4; u++) {
      const int b = 1 + 4 * g2 + u, pb = (b <= 2) ? 0 : b - 2;
      sc[u] = mfma16(K[b], Q[b], mcur);
      sp[u] = mfma16(K[pb], Q[b], b == 1 ? mh0 : (b == 2 ? mh1 : mprev));
    }
#pragma unroll
    for (int u = 0; u < 4; u++) {
#pragma unroll
      for (int r = 0; r < 4; r++) sc[u][r] = fmaxf(sc[u][r], sp[u][r]);
      mx[u] = fmaxf(fmaxf(sc[u][0], sc[u][1]), fmaxf(sc[u][2], sc[u][3]));
    }
#pragma unroll
    for (int u = 0; u < 4; u++) mx[u] = allq_max(mx[u]);
#pragma unroll
    for (int u = 0; u < 4; u++) {
      const int b = 1 + 4 * g2 + u;
      const float nm = -mx[u] * qs[b];
      f32x2 a = fma2(lo2(sc[u]), sp2(qs[b]), sp2(nm)), c = fma2(hi2(sc[u]), sp2(qs[b]), sp2(nm));
      pk[u] = u32x2{pack2(__builtin_amdgcn_exp2f(a[0]), __builtin_amdgcn_exp2f(a[1])), pack2(__builtin_amdgcn_exp2f(c[0]), __builtin_amdgcn_exp2f(c[1]))};
    }
#pragma unroll
    for (int u = 0; u < 4; u++) {
      const int b = 1 + 4 * g2 + u, pb = (b <= 2) ? 0 : b - 2;
      u32x4 pf = {pk[u][0] & ~selc[0], pk[u][1] & ~selc[1], pk[u][0] & selc[0], pk[u][1] & selc[1]};
      u32x4 vf = {V[pb][0], V[pb][1], V[b][0], V[b][1]};
      sm[u] = mfma16(ones, pk[u], zero);
      o[u] = mfma32(__builtin_bit_cast(s16x8, vf), __builtin_bit_cast(s16x8, pf), zero);
    }
#pragma unroll
    for (int g = 0; g < 2; g++) {
      u32x2 O[2];
#pragma unroll
      for (int par = 0; par < 2; par++) {
        const float inv = __builtin_amdgcn_rcpf(sm[2 * g + par][0]);
        O[par] = u32x2{pack2(lo2(o[2 * g + par]) * sp2(inv)), pack2(hi2(o[2 * g + par]) * sp2(inv))};
      }
      auto a = __builtin_amdgcn_permlane16_swap(O[0][0], O[1][0], false, false);
      auto b2 = __builtin_amdgcn_permlane16_swap(O[0][1], O[1][1], false, false);
      i32x4 v = {(int)a[0], (int)b2[0], (int)a[1], (int)b2[1]};
      const int rb = 12 * P + 4 * g2 + 2 * g;
      if (rb < 128) store16_sc1(v, voff, sa, (((rb >> 2) * 72 + (head >> 1)) * 4 + (rb & 3)) * 1024);
    }
  }
}

DEV constexpr int zpage(int j, int l) {
  return j == 0 ? 4 + l : j == 1 ? 8 + l : j == 2 ? 16 + l : j == 3 ? 12 + l : j == 4 ? (l < 2 ? 20 + l : 30 + l)
       : j == 5 ? 22 + l : j == 6 ? 34 + l : j == 7 ? 28 + l : 0 + l;
}

__global__ __launch_bounds__(512, 2) void fused_kernel(Params p) {
  __shared__ __attribute__((aligned(16))) char smem[LDS_BYTES];
  asm volatile("" :: "s"((unsigned)(size_t)(LDS char*)smem));
  const int w = __builtin_amdgcn_readfirstlane(threadIdx.x >> 6);
  const int bid = blockIdx.x, xcd = bid & 7, r = bid >> 3;
  STAMP(0);
  const bool is_mlp = IS_MLP_R(r);
  int P, S, rb0, nblk;
  if (is_mlp) { int idx = (xcd >> 1) * 21 + r; S = idx / 6; P = 6 * (xcd & 1) + idx % 6; rb0 = 32 * (P / 3) + 11 * (P % 3); nblk = (P % 3 == 2) ? 10 : 11; }
  else { int j = (xcd & 1) * 11 + (r - 21); P = j >> 1; S = 2 * (xcd >> 1) + (j & 1); rb0 = 12 * P; nblk = (P == 10) ? 8 : 12; }
  const int rt2 = 4 * xcd + (r & 3), ct2 = r >> 2;
  GLB u64* epoch = (GLB u64*)(p.ws + WS_EPOCH);
  GLB u64* flags = (GLB u64*)(p.ws + WS_FLAG);
  __amdgpu_buffer_rsrc_t ra = __builtin_amdgcn_make_buffer_rsrc(p.ws + WS_A, 0, (int)WS_A_BYTES, 0x00020000);
#define W2_OFFS() unsigned wb[4]; { const int krow = 4 * q + (i >> 2); \
    _Pragma("unroll") for (int cb = 0; cb < 4; cb++) wb[cb] = krow * 128 + ((cb ^ (krow >> 1)) & 3) * 32 + (i & 3) * 8; }

  if (w >= 4) {
    const int l = w - 4;
    const int lane = fresh_lane(), i = lane & 15, q = lane >> 4;
    W2_OFFS();
    const int rot = ROTG == 1 ? 0 : (((P + S) % ROTG) * (8 / ROTG));
    const i32x4 srd_x = make_srd(p.x, 2048 * 1024);
    const int RA = (is_mlp && nblk == 10) ? 20 : 24, RB = is_mlp ? 20 : 24;
    unsigned vx[2][3], lx[2];
    const u64 mxa = RA == 20 ? 0xffffffffull : ~0ull, mxb = RB == 20 ? 0xffffffffull : ~0ull;
#pragma unroll
    for (int h = 0; h < 2; h++) {
      const int row0 = h ? 4 * RA + l * RB : l * RA;
      lx[h] = row0 * 128;
#pragma unroll
      for (int j = 0; j < 3; j++) {
        int rho = row0 + 8 * j + (lane >> 3);
        int rb = rb0 + (rho >> 4); if (rb > 127) rb = 127;
        vx[h][j] = tok_of(rb, rho & 15) * 1024 + (((lane & 7) ^ (rho >> 1)) & 7) * 16;
      }
    }
    i32x4 srd_w0, srd_w1; unsigned vw[2], lw0, lwj, lw2, swj, sw2, wpitch32; u64 mw;
    if (is_mlp) {
      srd_w0 = make_srd(p.wg, 512 * 3584); srd_w1 = make_srd(p.wu, 512 * 3584);
      const int f = q | ((l & 1) << 2);
      vw[0] = vw[1] = (8 * l + q) * 3584 + S * 256 + (((i >> 1) ^ f) & 7) * 32 + (i & 1) * 16;
      lw0 = l * 2048; lwj = 1024; lw2 = 8192; swj = 4 * 3584; sw2 = 0; wpitch32 = 32 * 3584; mw = ~0ull;
    } else {
      srd_w0 = srd_w1 = make_srd(p.wqkv, 512 * 3072);
      const int row = lane >= 24 ? 1 : 0, rem = lane - 24 * row;
#pragma unroll
      for (int j = 0; j < 2; j++) {
        const int f = j | ((l & 1) << 1);
        vw[j] = (8 * l + row) * 3072 + (rem >> 3) * 1024 + S * 128 + ((((rem & 7) >> 1) ^ f) & 3) * 32 + (rem & 1) * 16;
      }
      lw0 = l * 3072; lwj = 768; lw2 = 1536; swj = 2 * 3072; sw2 = 4 * 3072; wpitch32 = 32 * 3072; mw = 0xffffffffffffull;
#pragma unroll
      for (int j = 0; j < 2; j++) {
        const int rho = 8 * j + (lane >> 3);
        int tok = 192 * P - 16 + 2 * (rho & 7) + j; if (tok < 0) tok = 0;
        const unsigned v = tok * 1024 + (((lane & 7) ^ (rho >> 1)) & 7) * 16;
        dma16(L_HALO + l * 2048 + j * 1024, v, srd_x, ((l + rot) & 7) * 128);
        dma16(L_HALO + (l + 4) * 2048 + j * 1024, v, srd_x, ((l + 4 + rot) & 7) * 128);
      }
    }
    auto issue_w = [&](int s) {
      const unsigned base = L_W + (s & 3) * 16384 + lw0, so = ((s + 2 * rot) & 15) * wpitch32;
      dma16m(base, vw[0], srd_w0, so, mw);
      dma16m(base + lwj, vw[1], srd_w0, so + swj, mw);
      dma16m(base + lw2, vw[0], srd_w1, so + sw2, mw);
      dma16m(base + lw2 + lwj, vw[1], srd_w1, so + sw2 + swj, mw);
    };
    auto issue_x = [&](int c, unsigned slotbase, int h) {
      const unsigned base = slotbase + lx[h], so = ((c + rot) & 7) * 128;
      dma16(base, vx[h][0], srd_x, so);
      dma16(base + 1024, vx[h][1], srd_x, so);
      dma16m(base + 2048, vx[h][2], srd_x, so, h ? mxb : mxa);
    };
    issue_x(0, L_X, 0); issue_x(0, L_X, 1); issue_w(0); issue_w(1); issue_x(1, L_X + XSLOT, 0);
    const unsigned xa = i * 128 + ((q ^ (i >> 1)) & 7) * 16;
    const int lastb = is_mlp ? nblk - 1 : 11;
    const unsigned b2off = ((l + 8 > lastb ? lastb : l + 8) - l) * 2048;
    float rs[4] = {0.f, 0.f, 0.f, 0.f}, rq[4] = {0.f, 0.f, 0.f, 0.f};
    const bool do_halo = !is_mlp && l == 0;
    u32x4 sf[4];
    auto stat_read = [&](unsigned xb, unsigned hb) {
      sf[0] = __builtin_bit_cast(u32x4, lds_b128(xb + l * 2048));
      sf[1] = __builtin_bit_cast(u32x4, lds_b128(xb + l * 2048 + 8192));
      sf[2] = __builtin_bit_cast(u32x4, lds_b128(xb + l * 2048 + b2off));
      if (do_halo) sf[3] = __builtin_bit_cast(u32x4, lds_b128(hb));
    };
    auto stat_acc = [&]() {
#pragma unroll
      for (int j = 0; j < 4; j++)
#pragma unroll
        for (int b = 0; b < 3; b++) { rs[b] = dot2(sf[b][j], 0x3f803f80u, rs[b]); rq[b] = dot2(sf[b][j], sf[b][j], rq[b]); }
      if (do_halo) {
#pragma unroll
        for (int j = 0; j < 4; j++) { rs[3] = dot2(sf[3][j], 0x3f803f80u, rs[3]); rq[3] = dot2(sf[3][j], sf[3][j], rq[3]); }
      }
    };
    if (lane < 16) *(LDS unsigned*)(size_t)(L_SYNC + lane * 4) = 0;
    sf[3] = u32x4{0, 0, 0, 0};
    waitl();
    waitv<7>(); bar();
    STAMP(1);
    unsigned x0 = L_X, x1 = L_X + XSLOT, x2 = L_X + 2 * XSLOT, hb = L_HALO;
#pragma unroll 1
    for (int c = 0; c < 6; c++) {
      stat_read(x0 + xa, hb + xa);
      issue_x(c + 1, x1, 1); issue_w(2 * c + 2);
      stat_acc();
      waitv<10>(); bar();
      stat_read(x0 + (xa ^ 64), hb + (xa ^ 64));
      issue_w(2 * c + 3); issue_x(c + 2, x2, 0);
      stat_acc();
      waitv<7>(); bar();
      unsigned t = x0; x0 = x1; x1 = x2; x2 = t; hb += 2048;
    }
    const i32x4 srd_o = make_srd(p.wo, 512 * 1024), srd_d = make_srd(p.wd, 1792 * 1024);
    unsigned voff_o, voff_d;
    {
      const int rr = lane >> 3; const unsigned col = ct2 * 128 + ((((lane & 7) >> 1) ^ (rr >> 1)) & 3) * 32 + (lane & 1) * 16;
      voff_d = rr * 1024 + col;
      voff_o = (8 * (rr >> 2) + (rr & 3)) * 1024 + col;
    }
    auto page_d = [&](int page, int hks) {
#pragma unroll
      for (int o = 0; o < 4; o++) dma16(page * 4096 + o * 1024, voff_d, srd_d, (hks * 32 + 8 * o) * 1024);
    };
    auto page_o = [&](int page, int ks) {
#pragma unroll
      for (int o = 0; o < 4; o++) dma16(page * 4096 + o * 1024, voff_o, srd_o, (ks * 32 + 16 * (o & 1) + 4 * (o >> 1)) * 1024);
    };
    auto issue_z = [&](int j) { if (j < 4) page_o(zpage(j, l), 4 * j + l); else page_d(zpage(j, l), 36 + 4 * (j - 4) + l); };
    auto issue_r = [&](int n) { page_d((n & 1) ? 28 + l : l, 9 * l + n); };
    s16x8 wr[9][4];
    auto read_r = [&](int n) {
      const unsigned pg = ((n & 1) ? 28 + l : l) * 4096;
#pragma unroll
      for (int cb = 0; cb < 4; cb++) wr[n][cb] = lds_tr(pg + wb[cb], pg + wb[cb] + 2048);
      waitl();
    };
    stat_read(x0 + xa, hb + xa);
    issue_x(7, x1, 1); issue_w(14);
    stat_acc();
    waitv<10>(); bar();
    stat_read(x0 + (xa ^ 64), hb + (xa ^ 64));
    issue_w(15); issue_r(0);
    stat_acc();
    waitv<8>(); bar();
    stat_read(x1 + xa, hb + 2048 + xa);
    issue_r(1); issue_z(0);
    stat_acc();
    waitv<12>(); bar();
    stat_read(x1 + (xa ^ 64), hb + 2048 + (xa ^ 64));
    waitv<8>(); read_r(0); issue_r(2); issue_z(1);
    stat_acc();
    bar();
    {
      float s = red4(rs[0], rs[1], rs[2], rs[3]), t = red4(rq[0], rq[1], rq[2], rq[3]);
      float m = s * (1.f / 512.f);
      float var = fmaxf(t * (1.f / 512.f) - m * m, 0.f);
      float sd = rbf(__builtin_amdgcn_sqrtf(rbf(rbf(var) + 1e-5f)));
      int blk = q == 3 ? 12 : l + 4 * q;
      if (q == 3 ? do_halo : (blk <= lastb)) {
        *(LDS float*)(size_t)(L_STAT + blk * 192 + i * 4) = -rbf(m);
        *(LDS float*)(size_t)(L_STAT + blk * 192 + 64 + i * 4) = sd;
        *(LDS float*)(size_t)(L_STAT + blk * 192 + 128 + i * 4) = __builtin_amdgcn_rcpf(sd) * (is_mlp ? -LOG2E : 1.f);
      }
    }
    waitv<12>(); read_r(1); issue_r(3); issue_z(2);
    waitl(); bar();
    STAMP(2);
    waitv<12>(); read_r(2); issue_r(4); issue_z(3);
    waitv<12>(); read_r(3); issue_r(5); issue_z(4);
    waitv<12>(); read_r(4); issue_r(6); issue_z(5);
    waitv<12>(); read_r(5);
    if (lane == 0) *(volatile LDS unsigned*)(size_t)(L_SYNC + 16 + l * 4) = 1;
    issue_r(7); issue_z(6);
    waitv<12>(); read_r(6); issue_r(8);
    waitv<8>(); read_r(7); issue_z(7);
    waitv<4>(); read_r(8); issue_z(8);
    waitv<0>();
    if (lane == 0) *(volatile LDS unsigned*)(size_t)(L_SYNC + 32 + l * 4) = 1;
    STAMP(3); STAMP(4); STAMP(5);
    {
      const u64 mytag = *(const LDS u64*)(size_t)(L_TAG + 63 * 8);
      const int b0 = 4 * rt2, b1 = b0 + 3;
      const int pl = 3 * (b0 >> 5) + (b0 & 31) / 11, ph = 3 * (b1 >> 5) + (b1 & 31) / 11;
      const int par = lane >= 14 ? 1 : 0, sl = lane - 14 * par;
      const bool need = lane < 28 && (par == (pl & 1) || par == (ph & 1)) && sl >= (9 * l) / 4 && sl <= (9 * l + 8) / 4;
      poll_flags(flags + bid * 64 + lane, need, mytag);
    }
    STAMP(6);
    f32x4 acc[4][4];
#pragma unroll
    for (int a = 0; a < 4; a++)
#pragma unroll
      for (int cb = 0; cb < 4; cb++) acc[a][cb] = f32x4{0.f, 0.f, 0.f, 0.f};
    {
      const unsigned abase = (rt2 * 72 + 16 + 9 * l) * 4096;
      constexpr int D = 8;
      i32x4 af[36];
#pragma unroll
      for (int n = 0; n < D; n++) af[n] = __builtin_amdgcn_raw_buffer_load_b128(ra, lane * 16 + (n & 3) * 1024, abase + (n >> 2) * 4096, 16);
#pragma unroll
      for (int n = 0; n < 36; n++) {
#pragma unroll
        for (int cb = 0; cb < 4; cb++) acc[n & 3][cb] = mfma32(wr[n >> 2][cb], __builtin_bit_cast(s16x8, af[n]), acc[n & 3][cb]);
        if (n + D < 36) af[n + D] = __builtin_amdgcn_raw_buffer_load_b128(ra, lane * 16 + ((n + D) & 3) * 1024, abase + ((n + D) >> 2) * 4096, 16);
        SB();
      }
    }
    STAMP(7);
    lds_spin4(L_SYNC + 48);
    {
      const unsigned pb = zpage(l == 0 ? 0 : l == 1 ? 1 : l == 2 ? 2 : 3, 0) * 4096;
#pragma unroll
      for (int a = 0; a < 4; a++)
#pragma unroll
        for (int cb = 0; cb < 4; cb++) *(LDS f32x4*)(size_t)(pb + (a * 4 + cb) * 1024 + lane * 16) = acc[a][cb];
    }
    waitl(); bar();
  } else {
    __builtin_amdgcn_s_setprio(1);
    if (w == 0) {
      const int lane = fresh_lane();
      int rt = (rb0 >> 2) + (lane >> 3), ct = lane & 7;
      bool pub = lane < 32 && rt <= ((rb0 + nblk - 1) >> 2);
      int cons = pub ? ((rt >> 2) + 8 * ((rt & 3) + 4 * ct)) : bid;
      u64 tg = __hip_atomic_load(epoch + cons * 8, __ATOMIC_RELAXED, __HIP_MEMORY_SCOPE_AGENT) + 1;
      *(LDS u64*)(size_t)(L_TAG + lane * 8) = tg;
      waitl();
    }
    if (is_mlp) {
      if (nblk == 11) mlp_path<11>(p, w, rb0, S TSPASS);
      else mlp_path<10>(p, w, rb0, S TSPASS);
    } else qkv_path(p, w, P, S TSPASS);
    STAMP(3);
    const int lane = fresh_lane(), i = lane & 15, q = lane >> 4;
    W2_OFFS();
    const u64 tg = *(const LDS u64*)(size_t)(L_TAG + lane * 8);
    const u64 mytag = *(const LDS u64*)(size_t)(L_TAG + 63 * 8);
    waitv<0>();
    unsigned old = 0;
    if (lane == 0) old = __hip_atomic_fetch_add((LDS unsigned*)(size_t)L_SYNC, 1u, __ATOMIC_RELAXED, __HIP_MEMORY_SCOPE_WORKGROUP);
    if (__builtin_amdgcn_readfirstlane(old) == 3) {
      int rt = (rb0 >> 2) + (lane >> 3), ct = lane & 7;
      if (lane < 32 && rt <= ((rb0 + nblk - 1) >> 2)) {
        int cons = (rt >> 2) + 8 * ((rt & 3) + 4 * ct);
        int slot = is_mlp ? ((P & 1) * 14 + S) : (28 + S);
        __hip_atomic_store(flags + cons * 64 + slot, tg, __ATOMIC_RELAXED, __HIP_MEMORY_SCOPE_AGENT);
      }
    }
    STAMP(4);
    const int otok = tok_of(4 * rt2 + w, i);
    u32x2 xres[4];
#pragma unroll
    for (int cb = 0; cb < 4; cb++) xres[cb] = *(const u32x2*)(p.x + otok * 1024 + ct2 * 128 + cb * 32 + q * 8);
    f32x4 as[4], ad[4];
#pragma unroll
    for (int cb = 0; cb < 4; cb++) { as[cb] = f32x4{0.f, 0.f, 0.f, 0.f}; ad[cb] = f32x4{0.f, 0.f, 0.f, 0.f}; }
    const unsigned abase = (rt2 * 72 * 4 + w) * 1024;
    poll_flags(flags + bid * 64 + lane, lane >= 28 && lane < 36, mytag);
    {
      i32x4 af[16];
#pragma unroll
      for (int n = 0; n < 16; n++) af[n] = __builtin_amdgcn_raw_buffer_load_b128(ra, lane * 16, abase + n * 4096, 16);
      lds_spin4(L_SYNC + 16);
#pragma unroll
      for (int n = 0; n < 16; n++) {
        const unsigned pg = zpage(n >> 2, n & 3) * 4096;
#pragma unroll
        for (int cb = 0; cb < 4; cb++) as[cb] = mfma32(lds_tr(pg + wb[cb], pg + wb[cb] + 2048), __builtin_bit_cast(s16x8, af[n]), as[cb]);
      }
    }
    waitl();
    if (lane == 0) *(volatile LDS unsigned*)(size_t)(L_SYNC + 48 + w * 4) = 1;
    STAMP(5);
    {
      const int b0 = 4 * rt2 + w;
      const int pl = 3 * (b0 >> 5) + (b0 & 31) / 11;
      const int lo = (pl & 1) * 14 + 9;
      poll_flags(flags + bid * 64 + lane, lane >= lo && lane < lo + 5, mytag);
    }
    STAMP(6);
    {
      i32x4 af[20];
#pragma unroll
      for (int n = 0; n < 20; n++) af[n] = __builtin_amdgcn_raw_buffer_load_b128(ra, lane * 16, abase + (52 + n) * 4096, 16);
      lds_spin4(L_SYNC + 32);
#pragma unroll
      for (int n = 0; n < 20; n++) {
        const unsigned pg = zpage(4 + (n >> 2), n & 3) * 4096;
#pragma unroll
        for (int cb = 0; cb < 4; cb++) ad[cb] = mfma32(lds_tr(pg + wb[cb], pg + wb[cb] + 2048), __builtin_bit_cast(s16x8, af[n]), ad[cb]);
      }
    }
    STAMP(7);
    bar();
    if (w == 0 && lane == 0) __hip_atomic_store(epoch + bid * 8, mytag, __ATOMIC_RELAXED, __HIP_MEMORY_SCOPE_AGENT);
#pragma unroll
    for (int cb = 0; cb < 4; cb++)
#pragma unroll
      for (int l = 0; l < 4; l++) ad[cb] += ldsf4(zpage(l, 0) * 4096 + (w * 4 + cb) * 1024 + lane * 16);
    unsigned o[4][2];
#pragma unroll
    for (int cb = 0; cb < 4; cb++)
#pragma unroll
      for (int h = 0; h < 2; h++) {
        f32x2 d = h ? hi2(ad[cb]) : lo2(ad[cb]), s = h ? hi2(as[cb]) : lo2(as[cb]);
        f32x2 e = d * sp2(-LOG2E);
        e[0] = __builtin_amdgcn_exp2f(e[0]); e[1] = __builtin_amdgcn_exp2f(e[1]);
        e = e + sp2(1.f);
        e[0] = __builtin_amdgcn_rcpf(e[0]); e[1] = __builtin_amdgcn_rcpf(e[1]);
        f32x2 xv = {__uint_as_float(xres[cb][h] << 16), __uint_as_float(xres[cb][h] & 0xffff0000u)};
        o[cb][h] = pack2(fma2(s * d, e, xv));
      }
#pragma unroll
    for (int c2 = 0; c2 < 2; c2++) {
      auto a = __builtin_amdgcn_permlane16_swap(o[2 * c2][0], o[2 * c2 + 1][0], false, false);
      auto b = __builtin_amdgcn_permlane16_swap(o[2 * c2][1], o[2 * c2 + 1][1], false, false);
      u32x4 v = {a[0], b[0], a[1], b[1]};
      *(u32x4*)(p.out + otok * 1024 + ct2 * 128 + c2 * 64 + (q & 1) * 32 + (q >> 1) * 16) = v;
    }
  }
  STAMP(8);
}

extern "C" void kernel_launch(void* const* d_in, const int* in_sizes, int n_in, void* d_out, int out_size,
                              void* d_ws, size_t ws_size, hipStream_t stream) {
  Params p;
  p.x = (const char*)d_in[0]; p.wqkv = (const char*)d_in[1]; p.wo = (const char*)d_in[2];
  p.wg = (const char*)d_in[4]; p.wu = (const char*)d_in[5]; p.wd = (const char*)d_in[6];
  p.out = (char*)d_out; p.ws = (char*)d_ws;
  fused_kernel<<<256, 512, 0, stream>>>(p);
}
```

```cpp
#define STAMPS
#include <hip/hip_runtime.h>
#include <hip/hip_bf16.h>

typedef short s16x4 __attribute__((ext_vector_type(4)));
typedef short s16x8 __attribute__((ext_vector_type(8)));
typedef __bf16 bf2 __attribute__((ext_vector_type(2)));
typedef float f32x2 __attribute__((ext_vector_type(2)));
typedef float f32x4 __attribute__((ext_vector_type(4)));
typedef int i32x4 __attribute__((ext_vector_type(4)));
typedef unsigned u32x2 __attribute__((ext_vector_type(2)));
typedef unsigned u32x4 __attribute__((ext_vector_type(4)));
typedef unsigned long long u64;
#define LDS __attribute__((address_space(3)))
#define GLB __attribute__((address_space(1)))
#define DEV __device__ __forceinline__
#define SB() __builtin_amdgcn_sched_barrier(0)

#define ROTG 1

constexpr int L_W = 0;
constexpr int L_X = 65536;
constexpr int XSLOT = 24576;
constexpr int L_HALO = 139264;
constexpr int L_MISC = 155648;
constexpr int L_STAT = L_MISC;
constexpr int L_CS = L_MISC + 2560;
constexpr int L_TAG = L_MISC + 3584;
constexpr int L_SYNC = L_MISC + 4096;
constexpr int LDS_BYTES = 163840;

constexpr size_t WS_A = 0;
constexpr size_t WS_A_BYTES = 32u * 72u * 4096u;
constexpr size_t WS_FLAG = WS_A_BYTES;
constexpr size_t WS_EPOCH = WS_FLAG + 256 * 512;
constexpr size_t WS_STAMP = 32u << 20;

#define IS_MLP_R(r) ((r) < 21)
struct Params {
  const char* x; const char* wqkv; const char* wo; const char* wg; const char* wu; const char* wd;
  char* out; char* ws;
};

DEV i32x4 make_srd(const void* p, unsigned bytes) {
  u64 a = (u64)p;
  i32x4 r; r[0] = (int)(unsigned)a; r[1] = (int)((unsigned)(a >> 32) & 0xffffu); r[2] = (int)bytes; r[3] = 0x00020000;
  return r;
}
DEV void dma16(unsigned lds, unsigned voff, i32x4 srd, unsigned soff) {
  asm volatile("s_mov_b32 m0, %0\n\ts_nop 0\n\tbuffer_load_dwordx4 %1, %2, %3 offen lds"
               :: "s"(lds), "v"(voff), "s"(srd), "s"(soff) : "memory");
}
DEV void dma16m(unsigned lds, unsigned voff, i32x4 srd, unsigned soff, u64 mask) {
  asm volatile("s_mov_b32 m0, %0\n\ts_mov_b64 exec, %4\n\tbuffer_load_dwordx4 %1, %2, %3 offen lds\n\ts_mov_b64 exec, -1"
               :: "s"(lds), "v"(voff), "s"(srd), "s"(soff), "s"(mask) : "memory");
}
DEV void store16_sc1(i32x4 v, unsigned voff, i32x4 srd, unsigned soff) {
  asm volatile("buffer_store_dwordx4 %0, %1, %2, %3 offen sc1\n\ts_nop 1" :: "v"(v), "v"(voff), "s"(srd), "s"(soff) : "memory");
}
template <int N> DEV void waitv() { asm volatile("s_waitcnt vmcnt(%0)" :: "n"(N) : "memory"); }
DEV void waitl() { __builtin_amdgcn_s_waitcnt(0xC07F); asm volatile("" ::: "memory"); }
DEV void bar() { asm volatile("s_barrier" ::: "memory"); }
DEV s16x8 lds_b128(unsigned a) { return *(const LDS s16x8*)(size_t)a; }
DEV s16x4 lds_tr4(unsigned a) { return __builtin_amdgcn_ds_read_tr16_b64_v4i16((LDS s16x4*)(size_t)a); }
DEV s16x8 cat(s16x4 lo, s16x4 hi) { return __builtin_shufflevector(lo, hi, 0, 1, 2, 3, 4, 5, 6, 7); }
DEV s16x8 lds_tr(unsigned a0, unsigned a1) { return cat(lds_tr4(a0), lds_tr4(a1)); }
DEV f32x4 ldsf4(unsigned a) { return *(const LDS f32x4*)(size_t)a; }
DEV float ldsf(unsigned a) { return *(const LDS float*)(size_t)a; }
DEV f32x4 mfma32(s16x8 a, s16x8 b, f32x4 c) { return __builtin_amdgcn_mfma_f32_16x16x32_bf16(a, b, c, 0, 0, 0); }
DEV f32x4 mfma16(u32x2 a, u32x2 b, f32x4 c) {
  return __builtin_amdgcn_mfma_f32_16x16x16bf16_1k(__builtin_bit_cast(s16x4, a), __builtin_bit_cast(s16x4, b), c, 0, 0, 0);
}
DEV float dot2(unsigned a, unsigned b, float c) {
  return __builtin_amdgcn_fdot2_f32_bf16(__builtin_bit_cast(bf2, a), __builtin_bit_cast(bf2, b), c, false);
}
DEV unsigned pack2(float a, float b) { bf2 p = {(__bf16)a, (__bf16)b}; return __builtin_bit_cast(unsigned, p); }
DEV unsigned pack2(f32x2 a) { return pack2(a[0], a[1]); }
DEV float rbf(float a) { return (float)(__bf16)a; }
DEV f32x2 lo2(f32x4 a) { return f32x2{a[0], a[1]}; }
DEV f32x2 hi2(f32x4 a) { return f32x2{a[2], a[3]}; }
DEV f32x2 fma2(f32x2 a, f32x2 b, f32x2 c) { return __builtin_elementwise_fma(a, b, c); }
DEV f32x2 sp2(float a) { return f32x2{a, a}; }
DEV float red4(float v0, float v1, float v2, float v3) {
  auto a = __builtin_amdgcn_permlane32_swap(__float_as_uint(v0), __float_as_uint(v2), false, false);
  auto b = __builtin_amdgcn_permlane32_swap(__float_as_uint(v1), __float_as_uint(v3), false, false);
  float s02 = __uint_as_float(a[0]) + __uint_as_float(a[1]);
  float s13 = __uint_as_float(b[0]) + __uint_as_float(b[1]);
  auto c = __builtin_amdgcn_permlane16_swap(__float_as_uint(s02), __float_as_uint(s13), false, false);
  return __uint_as_float(c[0]) + __uint_as_float(c[1]);
}
DEV float allq_max(float v) {
  auto a = __builtin_amdgcn_permlane32_swap(__float_as_uint(v), __float_as_uint(v), false, false);
  v = fmaxf(__uint_as_float(a[0]), __uint_as_float(a[1]));
  auto b = __builtin_amdgcn_permlane16_swap(__float_as_uint(v), __float_as_uint(v), false, false);
  return fmaxf(__uint_as_float(b[0]), __uint_as_float(b[1]));
}
DEV int fresh_lane() { int l = __builtin_amdgcn_mbcnt_hi(~0u, __builtin_amdgcn_mbcnt_lo(~0u, 0u)); asm volatile("" : "+v"(l)); return l; }
DEV int tok_of(int rb, int i) { return 32 * (rb >> 1) + 2 * i + (rb & 1); }
DEV void lds_spin4(unsigned a) {
  for (;;) {
    u32x4 v = *(volatile LDS u32x4*)(size_t)a;
    if (__builtin_amdgcn_readfirstlane(v[0] & v[1] & v[2] & v[3])) break;
    __builtin_amdgcn_s_sleep(1);
  }
  asm volatile("" ::: "memory");
}
DEV void poll_flags(GLB u64* fl, bool need, u64 tag) {
  unsigned spins = 0;
  for (;;) {
    u64 v = __hip_atomic_load(fl, __ATOMIC_RELAXED, __HIP_MEMORY_SCOPE_AGENT);
    if (__all(!need || v == tag)) break;
    if (++spins > (1u << 20)) break;
    __builtin_amdgcn_s_sleep(1);
  }
  __builtin_amdgcn_fence(__ATOMIC_ACQUIRE, "wavefront");
  asm volatile("" ::: "memory");
}

#define STAMP(n) do { SB(); asm volatile("s_memrealtime %0\n\ts_waitcnt lgkmcnt(0)" : "=s"(ts[n]) :: "memory"); SB(); } while (0)
#define TSARG , u64 (&ts)[12]
#define TSPASS , ts

constexpr float LOG2E = 1.4426950408889634f;

template <bool QKV, int NB, int NC>
struct S1 { f32x4 acc[NB][NC]; float cs[NC]; };

template <bool QKV> DEV s16x4 w_half(int idx, unsigned wa0, unsigned wa1) {
  const int c = idx >> 1, t = idx & 1;
  if (QKV) return lds_tr4(wa0 + c * 128 + t * 4 * 384);
  return lds_tr4(((c & 1) ? wa1 : wa0) + (c >> 1) * 8192 + t * 1024);
}
template <bool QKV> DEV s16x8 x_frag(int b, unsigned xa, unsigned ha) {
  if (QKV) return b == 0 ? lds_b128(ha) : lds_b128(xa + (b - 1) * 2048);
  return lds_b128(xa + b * 2048);
}
constexpr int PF = 3;
template <bool QKV, int NB, int NC, bool LAST>
DEV void c_step(S1<QKV, NB, NC>& st, s16x8 (&wf)[NC], s16x8 (&xp)[PF],
                unsigned xa, unsigned ha, unsigned xan, unsigned han, unsigned wn0, unsigned wn1) {
  s16x8 xf[NB + PF]; s16x4 wh[2 * NC];
#pragma unroll
  for (int j = 0; j < PF; j++) xf[j] = xp[j];
#pragma unroll
  for (int b = 0; b < NB; b++) {
    if (!LAST && b < NC) { wh[2 * b] = w_half<QKV>(2 * b, wn0, wn1); wh[2 * b + 1] = w_half<QKV>(2 * b + 1, wn0, wn1); }
    if (b + PF < NB) xf[b + PF] = x_frag<QKV>(b + PF, xa, ha);
    else if (!LAST) xf[b + PF] = x_frag<QKV>(b + PF - NB, xan, han);
#pragma unroll
    for (int c = 0; c < NC; c++) {
      if (QKV && b == 0 && c == 0) continue;
      if (QKV && c == 2) st.acc[b][c] = mfma32(xf[b], wf[c], st.acc[b][c]);
      else st.acc[b][c] = mfma32(wf[c], xf[b], st.acc[b][c]);
    }
    if (b < 2 * NC) {
      u32x4 u = __builtin_bit_cast(u32x4, wf[b >> 1]);
      st.cs[b >> 1] = dot2(u[2 * (b & 1)], 0x3f803f80u, st.cs[b >> 1]);
      st.cs[b >> 1] = dot2(u[2 * (b & 1) + 1], 0x3f803f80u, st.cs[b >> 1]);
    }
    SB();
  }
  if (!LAST) {
#pragma unroll
    for (int c = 0; c < NC; c++) wf[c] = cat(wh[2 * c], wh[2 * c + 1]);
#pragma unroll
    for (int j = 0; j < PF; j++) xp[j] = xf[NB + j];
  }
}
template <bool QKV, int NB, int NC>
DEV void c_main(S1<QKV, NB, NC>& st, unsigned xa, unsigned wa0, unsigned wa1 TSARG) {
#pragma unroll
  for (int b = 0; b < NB; b++)
#pragma unroll
    for (int c = 0; c < NC; c++) st.acc[b][c] = f32x4{0.f, 0.f, 0.f, 0.f};
#pragma unroll
  for (int c = 0; c < NC; c++) st.cs[c] = 0.f;
  s16x8 wf[NC], xp[PF];
  bar();
  STAMP(1);
#pragma unroll
  for (int c = 0; c < NC; c++) wf[c] = cat(w_half<QKV>(2 * c, L_W + wa0, L_W + wa1), w_half<QKV>(2 * c + 1, L_W + wa0, L_W + wa1));
#pragma unroll
  for (int j = 0; j < PF; j++) xp[j] = x_frag<QKV>(j, L_X + xa, L_HALO + xa);
  waitl();
  unsigned xb = L_X, hb = L_HALO, wb = 0;
#pragma unroll 1
  for (int it = 0; it < 8; it++) {
    unsigned xbn = (xb == L_X + 2 * XSLOT) ? L_X : xb + XSLOT;
    bar();
    c_step<QKV, NB, NC, false>(st, wf, xp, xb + xa, hb + xa, xb + (xa ^ 64), hb + (xa ^ 64), wb + 16384 + wa0, wb + 16384 + wa1);
    wb ^= 32768;
    bar();
    c_step<QKV, NB, NC, false>(st, wf, xp, xb + (xa ^ 64), hb + (xa ^ 64), xbn + xa, hb + 2048 + xa, wb + wa0, wb + wa1);
    xb = xbn; hb += 2048;
  }
}

template <int NB>
DEV void mlp_path(const Params& p, int w, int rb0, int S TSARG) {
  int lane = fresh_lane(), i = lane & 15, q = lane >> 4;
  S1<false, NB, 4> st;
  const unsigned xa = i * 128 + ((q ^ (i >> 1)) & 7) * 16;
  const int krow = 8 * q + (i >> 2);
  const int f = (krow & 3) | (((krow >> 3) & 1) << 2);
  const unsigned wa0 = krow * 256 + (((2 * w) ^ f) & 7) * 32 + (i & 3) * 8;
  c_main<false, NB, 4>(st, xa, wa0, wa0 ^ 32 TSPASS);
  lane = fresh_lane(); i = lane & 15; q = lane >> 4;
  *(LDS float*)(size_t)(L_CS + w * 256 + lane * 4) = red4(st.cs[0], st.cs[1], st.cs[2], st.cs[3]);
  bar();
  STAMP(2);
  f32x4 cs[4];
#pragma unroll
  for (int c = 0; c < 4; c++) cs[c] = ldsf4(L_CS + w * 256 + c * 64 + q * 16);
  float nm[NB + 1], sd[NB + 1], c1[NB + 1];
  nm[0] = ldsf(L_STAT + i * 4); sd[0] = ldsf(L_STAT + 64 + i * 4); c1[0] = ldsf(L_STAT + 128 + i * 4);
  const i32x4 sa = make_srd(p.ws + WS_A, (unsigned)WS_A_BYTES);
  const int ks = 16 + 4 * S + w;
#pragma unroll
  for (int b = 0; b < NB; b++) {
    f32x2 z[4], u[4];
    if (b + 1 < NB) { nm[b + 1] = ldsf(L_STAT + (b + 1) * 192 + i * 4); sd[b + 1] = ldsf(L_STAT + (b + 1) * 192 + 64 + i * 4); c1[b + 1] = ldsf(L_STAT + (b + 1) * 192 + 128 + i * 4); }
#pragma unroll
    for (int j = 0; j < 4; j++) {
      f32x2 a = (j & 1) ? hi2(st.acc[b][j >> 1]) : lo2(st.acc[b][j >> 1]);
      f32x2 c = (j & 1) ? hi2(cs[j >> 1]) : lo2(cs[j >> 1]);
      z[j] = fma2(c, sp2(nm[b]), a);
    }
    SB();
#pragma unroll
    for (int j = 0; j < 4; j++) z[j] = z[j] * sp2(c1[b]);
    SB();
#pragma unroll
    for (int j = 0; j < 4; j++) {
      z[j][0] = __builtin_amdgcn_exp2f(z[j][0]); z[j][1] = __builtin_amdgcn_exp2f(z[j][1]);
      f32x2 a = (j & 1) ? hi2(st.acc[b][2 + (j >> 1)]) : lo2(st.acc[b][2 + (j >> 1)]);
      f32x2 c = (j & 1) ? hi2(cs[2 + (j >> 1)]) : lo2(cs[2 + (j >> 1)]);
      u[j] = fma2(c, sp2(nm[b]), a);
    }
    SB();
#pragma unroll
    for (int j = 0; j < 4; j++) z[j] = fma2(z[j], sp2(sd[b]), sp2(sd[b]));
    SB();
#pragma unroll
    for (int j = 0; j < 4; j++) { z[j][0] = __builtin_amdgcn_rcpf(z[j][0]); z[j][1] = __builtin_amdgcn_rcpf(z[j][1]); }
    SB();
#pragma unroll
    for (int j = 0; j < 4; j++) u[j] = u[j] * z[j];
    SB();
    i32x4 v = {(int)pack2(u[0]), (int)pack2(u[1]), (int)pack2(u[2]), (int)pack2(u[3])};
    const int rb = rb0 + b;
    store16_sc1(v, lane * 16, sa, (((rb >> 2) * 72 + ks) * 4 + (rb & 3)) * 1024);
  }
}

DEV void qkv_path(const Params& p, int w, int P, int G TSARG) {
  int lane = fresh_lane(), i = lane & 15, q = lane >> 4;
  S1<true, 13, 3> st;
  const unsigned xa = i * 128 + ((q ^ (i >> 1)) & 7) * 16;
  const int krow = 8 * q + (i >> 2);
  const int f = ((krow >> 1) & 1) | (((krow >> 3) & 1) << 1);
  const unsigned wa0 = krow * 384 + ((w ^ f) & 3) * 32 + (i & 3) * 8;
  c_main<true, 13, 3>(st, xa, wa0, wa0 TSPASS);
  lane = fresh_lane(); i = lane & 15; q = lane >> 4;
  *(LDS float*)(size_t)(L_CS + w * 256 + lane * 4) = red4(st.cs[0], st.cs[1], st.cs[2], st.cs[2]);
  bar();
  STAMP(2);
  const f32x4 csq = ldsf4(L_CS + w * 256 + q * 16), csk = ldsf4(L_CS + w * 256 + 64 + q * 16);
  const float ncsv = ldsf(L_CS + w * 256 + 128 + i * 4);
  u32x2 Q[13], K[13], V[13]; float qs[13];
  auto fold = [&](int b) {
    const int tb = L_STAT + (b == 0 ? 12 : b - 1) * 192;
    const float nm = ldsf(tb + i * 4), rstd = ldsf(tb + 128 + i * 4);
    const f32x4 m4 = ldsf4(tb + q * 16), r4 = ldsf4(tb + 128 + q * 16);
    if (b > 0) {
      Q[b] = u32x2{pack2(fma2(lo2(csq), sp2(nm), lo2(st.acc[b][0]))), pack2(fma2(hi2(csq), sp2(nm), hi2(st.acc[b][0])))};
      qs[b] = rstd * (0.25f * LOG2E);
    }
    K[b] = u32x2{pack2(fma2(lo2(csk), sp2(nm), lo2(st.acc[b][1])) * sp2(rstd)), pack2(fma2(hi2(csk), sp2(nm), hi2(st.acc[b][1])) * sp2(rstd))};
    V[b] = u32x2{pack2(fma2(lo2(m4), sp2(ncsv), lo2(st.acc[b][2])) * lo2(r4)), pack2(fma2(hi2(m4), sp2(ncsv), hi2(st.acc[b][2])) * hi2(r4))};
  };
  fold(0);
  const float NEG = -__builtin_inff();
  f32x4 mcur, mprev, mh0, mh1; u32x2 selc;
  const bool first = (P == 0);
#pragma unroll
  for (int r = 0; r < 4; r++) {
    int kap = 4 * q + r;
    mcur[r] = (kap <= i && kap >= i - 7) ? 0.f : NEG;
    mprev[r] = (kap >= i + 9) ? 0.f : NEG;
    mh0[r] = (!first && kap < 8 && kap >= i + 1) ? 0.f : NEG;
    mh1[r] = (!first && kap >= i + 9) ? 0.f : NEG;
  }
  selc[0] = ((4 * q <= i) ? 0xffffu : 0u) | ((4 * q + 1 <= i) ? 0xffff0000u : 0u);
  selc[1] = ((4 * q + 2 <= i) ? 0xffffu : 0u) | ((4 * q + 3 <= i) ? 0xffff0000u : 0u);
  const i32x4 sa = make_srd(p.ws + WS_A, (unsigned)WS_A_BYTES);
  const int head = 4 * G + w;
  const unsigned voff = (q & 1) * 1024 + ((2 * (head & 1) + (q >> 1)) * 16 + i) * 16;
  const u32x2 ones = {0x3f803f80u, 0x3f803f80u};
  const f32x4 zero = {0.f, 0.f, 0.f, 0.f};
#pragma unroll
  for (int g2 = 0; g2 < 3; g2++) {
    f32x4 sc[4], sp[4]; float mx[4]; u32x2 pk[4]; f32x4 o[4], sm[4];
#pragma unroll
    for (int u = 0; u < 4; u++) fold(1 + 4 * g2 + u);
#pragma unroll
    for (int u = 0; u < 4; u++) {
      const int b = 1 + 4 * g2 + u, pb = (b <= 2) ? 0 : b - 2;
      sc[u] = mfma16(K[b], Q[b], mcur);
      sp[u] = mfma16(K[pb], Q[b], b == 1 ? mh0 : (b == 2 ? mh1 : mprev));
    }
#pragma unroll
    for (int u = 0; u < 4; u++) {
#pragma unroll
      for (int r = 0; r < 4; r++) sc[u][r] = fmaxf(sc[u][r], sp[u][r]);
      mx[u] = fmaxf(fmaxf(sc[u][0], sc[u][1]), fmaxf(sc[u][2], sc[u][3]));
    }
#pragma unroll
    for (int u = 0; u < 4; u++) mx[u] = allq_max(mx[u]);
#pragma unroll
    for (int u = 0; u < 4; u++) {
      const int b = 1 + 4 * g2 + u;
      const float nm = -mx[u] * qs[b];
      f32x2 a = fma2(lo2(sc[u]), sp2(qs[b]), sp2(nm)), c = fma2(hi2(sc[u]), sp2(qs[b]), sp2(nm));
      pk[u] = u32x2{pack2(__builtin_amdgcn_exp2f(a[0]), __builtin_amdgcn_exp2f(a[1])), pack2(__builtin_amdgcn_exp2f(c[0]), __builtin_amdgcn_exp2f(c[1]))};
    }
#pragma unroll
    for (int u = 0; u < 4; u++) {
      const int b = 1 + 4 * g2 + u, pb = (b <= 2) ? 0 : b - 2;
      u32x4 pf = {pk[u][0] & ~selc[0], pk[u][1] & ~selc[1], pk[u][0] & selc[0], pk[u][1] & selc[1]};
      u32x4 vf = {V[pb][0], V[pb][1], V[b][0], V[b][1]};
      sm[u] = mfma16(ones, pk[u], zero);
      o[u] = mfma32(__builtin_bit_cast(s16x8, vf), __builtin_bit_cast(s16x8, pf), zero);
    }
#pragma unroll
    for (int g = 0; g < 2; g++) {
      u32x2 O[2];
#pragma unroll
      for (int par = 0; par < 2; par++) {
        const float inv = __builtin_amdgcn_rcpf(sm[2 * g + par][0]);
        O[par] = u32x2{pack2(lo2(o[2 * g + par]) * sp2(inv)), pack2(hi2(o[2 * g + par]) * sp2(inv))};
      }
      auto a = __builtin_amdgcn_permlane16_swap(O[0][0], O[1][0], false, false);
      auto b2 = __builtin_amdgcn_permlane16_swap(O[0][1], O[1][1], false, false);
      i32x4 v = {(int)a[0], (int)b2[0], (int)a[1], (int)b2[1]};
      const int rb = 12 * P + 4 * g2 + 2 * g;
      if (rb < 128) store16_sc1(v, voff, sa, (((rb >> 2) * 72 + (head >> 1)) * 4 + (rb & 3)) * 1024);
    }
  }
}

DEV constexpr int zpage(int j, int l) {
  return j == 0 ? 4 + l : j == 1 ? 8 + l : j == 2 ? 16 + l : j == 3 ? 12 + l : j == 4 ? (l < 2 ? 20 + l : 30 + l)
       : j == 5 ? 22 + l : j == 6 ? 34 + l : j == 7 ? 28 + l : 0 + l;
}

__global__ __launch_bounds__(512, 2) void fused_kernel(Params p) {
  __shared__ __attribute__((aligned(16))) char smem[LDS_BYTES];
  asm volatile("" :: "s"((unsigned)(size_t)(LDS char*)smem));
  const int w = __builtin_amdgcn_readfirstlane(threadIdx.x >> 6);
  const int bid = blockIdx.x, xcd = bid & 7, r = bid >> 3;
  u64 ts[12]; u64 cyc0 = __builtin_amdgcn_s_memtime();
  STAMP(0);
  const bool is_mlp = IS_MLP_R(r);
  int P, S, rb0, nblk;
  if (is_mlp) { int idx = (xcd >> 1) * 21 + r; S = idx / 6; P = 6 * (xcd & 1) + idx % 6; rb0 = 32 * (P / 3) + 11 * (P % 3); nblk = (P % 3 == 2) ? 10 : 11; }
  else { int j = (xcd & 1) * 11 + (r - 21); P = j >> 1; S = 2 * (xcd >> 1) + (j & 1); rb0 = 12 * P; nblk = (P == 10) ? 8 : 12; }
  const int rt2 = 4 * xcd + (r & 3), ct2 = r >> 2;
  GLB u64* epoch = (GLB u64*)(p.ws + WS_EPOCH);
  GLB u64* flags = (GLB u64*)(p.ws + WS_FLAG);
  __amdgpu_buffer_rsrc_t ra = __builtin_amdgcn_make_buffer_rsrc(p.ws + WS_A, 0, (int)WS_A_BYTES, 0x00020000);
#define W2_OFFS() unsigned wb[4]; { const int krow = 4 * q + (i >> 2); \
    _Pragma("unroll") for (int cb = 0; cb < 4; cb++) wb[cb] = krow * 128 + ((cb ^ (krow >> 1)) & 3) * 32 + (i & 3) * 8; }

  if (w >= 4) {
    const int l = w - 4;
    const int lane = fresh_lane(), i = lane & 15, q = lane >> 4;
    W2_OFFS();
    const int rot = ROTG == 1 ? 0 : (((P + S) % ROTG) * (8 / ROTG));
    const i32x4 srd_x = make_srd(p.x, 2048 * 1024);
    const int RA = (is_mlp && nblk == 10) ? 20 : 24, RB = is_mlp ? 20 : 24;
    unsigned vx[2][3], lx[2];
    const u64 mxa = RA == 20 ? 0xffffffffull : ~0ull, mxb = RB == 20 ? 0xffffffffull : ~0ull;
#pragma unroll
    for (int h = 0; h < 2; h++) {
      const int row0 = h ? 4 * RA + l * RB : l * RA;
      lx[h] = row0 * 128;
#pragma unroll
      for (int j = 0; j < 3; j++) {
        int rho = row0 + 8 * j + (lane >> 3);
        int rb = rb0 + (rho >> 4); if (rb > 127) rb = 127;
        vx[h][j] = tok_of(rb, rho & 15) * 1024 + (((lane & 7) ^ (rho >> 1)) & 7) * 16;
      }
    }
    i32x4 srd_w0, srd_w1; unsigned vw[2], lw0, lwj, lw2, swj, sw2, wpitch32; u64 mw;
    if (is_mlp) {
      srd_w0 = make_srd(p.wg, 512 * 3584); srd_w1 = make_srd(p.wu, 512 * 3584);
      const int f = q | ((l & 1) << 2);
      vw[0] = vw[1] = (8 * l + q) * 3584 + S * 256 + (((i >> 1) ^ f) & 7) * 32 + (i & 1) * 16;
      lw0 = l * 2048; lwj = 1024; lw2 = 8192; swj = 4 * 3584; sw2 = 0; wpitch32 = 32 * 3584; mw = ~0ull;
    } else {
      srd_w0 = srd_w1 = make_srd(p.wqkv, 512 * 3072);
      const int row = lane >= 24 ? 1 : 0, rem = lane - 24 * row;
#pragma unroll
      for (int j = 0; j < 2; j++) {
        const int f = j | ((l & 1) << 1);
        vw[j] = (8 * l + row) * 3072 + (rem >> 3) * 1024 + S * 128 + ((((rem & 7) >> 1) ^ f) & 3) * 32 + (rem & 1) * 16;
      }
      lw0 = l * 3072; lwj = 768; lw2 = 1536; swj = 2 * 3072; sw2 = 4 * 3072; wpitch32 = 32 * 3072; mw = 0xffffffffffffull;
#pragma unroll
      for (int j = 0; j < 2; j++) {
        const int rho = 8 * j + (lane >> 3);
        int tok = 192 * P - 16 + 2 * (rho & 7) + j; if (tok < 0) tok = 0;
        const unsigned v = tok * 1024 + (((lane & 7) ^ (rho >> 1)) & 7) * 16;
        dma16(L_HALO + l * 2048 + j * 1024, v, srd_x, ((l + rot) & 7) * 128);
        dma16(L_HALO + (l + 4) * 2048 + j * 1024, v, srd_x, ((l + 4 + rot) & 7) * 128);
      }
    }
    auto issue_w = [&](int s) {
      const unsigned base = L_W + (s & 3) * 16384 + lw0, so = ((s + 2 * rot) & 15) * wpitch32;
      dma16m(base, vw[0], srd_w0, so, mw);
      dma16m(base + lwj, vw[1], srd_w0, so + swj, mw);
      dma16m(base + lw2, vw[0], srd_w1, so + sw2, mw);
      dma16m(base + lw2 + lwj, vw[1], srd_w1, so + sw2 + swj, mw);
    };
    auto issue_x = [&](int c, unsigned slotbase, int h) {
      const unsigned base = slotbase + lx[h], so = ((c + rot) & 7) * 128;
      dma16(base, vx[h][0], srd_x, so);
      dma16(base + 1024, vx[h][1], srd_x, so);
      dma16m(base + 2048, vx[h][2], srd_x, so, h ? mxb : mxa);
    };
    issue_x(0, L_X, 0); issue_x(0, L_X, 1); issue_w(0); issue_w(1); issue_x(1, L_X + XSLOT, 0);
    const unsigned xa = i * 128 + ((q ^ (i >> 1)) & 7) * 16;
    const int lastb = is_mlp ? nblk - 1 : 11;
    const unsigned b2off = ((l + 8 > lastb ? lastb : l + 8) - l) * 2048;
    float rs[4] = {0.f, 0.f, 0.f, 0.f}, rq[4] = {0.f, 0.f, 0.f, 0.f};
    const bool do_halo = !is_mlp && l == 0;
    u32x4 sf[4];
    auto stat_read = [&](unsigned xb, unsigned hb) {
      sf[0] = __builtin_bit_cast(u32x4, lds_b128(xb + l * 2048));
      sf[1] = __builtin_bit_cast(u32x4, lds_b128(xb + l * 2048 + 8192));
      sf[2] = __builtin_bit_cast(u32x4, lds_b128(xb + l * 2048 + b2off));
      if (do_halo) sf[3] = __builtin_bit_cast(u32x4, lds_b128(hb));
    };
    auto stat_acc = [&]() {
#pragma unroll
      for (int j = 0; j < 4; j++)
#pragma unroll
        for (int b = 0; b < 3; b++) { rs[b] = dot2(sf[b][j], 0x3f803f80u, rs[b]); rq[b] = dot2(sf[b][j], sf[b][j], rq[b]); }
      if (do_halo) {
#pragma unroll
        for (int j = 0; j < 4; j++) { rs[3] = dot2(sf[3][j], 0x3f803f80u, rs[3]); rq[3] = dot2(sf[3][j], sf[3][j], rq[3]); }
      }
    };
    if (lane < 16) *(LDS unsigned*)(size_t)(L_SYNC + lane * 4) = 0;
    sf[3] = u32x4{0, 0, 0, 0};
    waitl();
    waitv<7>(); bar();
    STAMP(1);
    unsigned x0 = L_X, x1 = L_X + XSLOT, x2 = L_X + 2 * XSLOT, hb = L_HALO;
#pragma unroll 1
    for (int c = 0; c < 6; c++) {
      stat_read(x0 + xa, hb + xa);
      issue_x(c + 1, x1, 1); issue_w(2 * c + 2);
      stat_acc();
      waitv<10>(); bar();
      stat_read(x0 + (xa ^ 64), hb + (xa ^ 64));
      issue_w(2 * c + 3); issue_x(c + 2, x2, 0);
      stat_acc();
      waitv<7>(); bar();
      unsigned t = x0; x0 = x1; x1 = x2; x2 = t; hb += 2048;
    }
    const i32x4 srd_o = make_srd(p.wo, 512 * 1024), srd_d = make_srd(p.wd, 1792 * 1024);
    unsigned voff_o, voff_d;
    {
      const int rr = lane >> 3; const unsigned col = ct2 * 128 + ((((lane & 7) >> 1) ^ (rr >> 1)) & 3) * 32 + (lane & 1) * 16;
      voff_d = rr * 1024 + col;
      voff_o = (8 * (rr >> 2) + (rr & 3)) * 1024 + col;
    }
    auto page_d = [&](int page, int hks) {
#pragma unroll
      for (int o = 0; o < 4; o++) dma16(page * 4096 + o * 1024, voff_d, srd_d, (hks * 32 + 8 * o) * 1024);
    };
    auto page_o = [&](int page, int ks) {
#pragma unroll
      for (int o = 0; o < 4; o++) dma16(page * 4096 + o * 1024, voff_o, srd_o, (ks * 32 + 16 * (o & 1) + 4 * (o >> 1)) * 1024);
    };
    auto issue_z = [&](int j) { if (j < 4) page_o(zpage(j, l), 4 * j + l); else page_d(zpage(j, l), 36 + 4 * (j - 4) + l); };
    auto issue_r = [&](int n) { page_d((n & 1) ? 28 + l : l, 9 * l + n); };
    s16x8 wr[9][4];
    auto read_r = [&](int n) {
      const unsigned pg = ((n & 1) ? 28 + l : l) * 4096;
#pragma unroll
      for (int cb = 0; cb < 4; cb++) wr[n][cb] = lds_tr(pg + wb[cb], pg + wb[cb] + 2048);
      waitl();
    };
    stat_read(x0 + xa, hb + xa);
    issue_x(7, x1, 1); issue_w(14);
    stat_acc();
    waitv<10>(); bar();
    stat_read(x0 + (xa ^ 64), hb + (xa ^ 64));
    issue_w(15); issue_r(0);
    stat_acc();
    waitv<8>(); bar();
    stat_read(x1 + xa, hb + 2048 + xa);
    issue_r(1); issue_z(0);
    stat_acc();
    waitv<12>(); bar();
    stat_read(x1 + (xa ^ 64), hb + 2048 + (xa ^ 64));
    waitv<8>(); read_r(0); issue_r(2); issue_z(1);
    stat_acc();
    bar();
    {
      float s = red4(rs[0], rs[1], rs[2], rs[3]), t = red4(rq[0], rq[1], rq[2], rq[3]);
      float m = s * (1.f / 512.f);
      float var = fmaxf(t * (1.f / 512.f) - m * m, 0.f);
      float sd = rbf(__builtin_amdgcn_sqrtf(rbf(rbf(var) + 1e-5f)));
      int blk = q == 3 ? 12 : l + 4 * q;
      if (q == 3 ? do_halo : (blk <= lastb)) {
        *(LDS float*)(size_t)(L_STAT + blk * 192 + i * 4) = -rbf(m);
        *(LDS float*)(size_t)(L_STAT + blk * 192 + 64 + i * 4) = sd;
        *(LDS float*)(size_t)(L_STAT + blk * 192 + 128 + i * 4) = __builtin_amdgcn_rcpf(sd) * (is_mlp ? -LOG2E : 1.f);
      }
    }
    waitv<12>(); read_r(1); issue_r(3); issue_z(2);
    waitl(); bar();
    STAMP(2);
    waitv<12>(); read_r(2); issue_r(4); issue_z(3);
    waitv<12>(); read_r(3); issue_r(5); issue_z(4);
    waitv<12>(); read_r(4); issue_r(6); issue_z(5);
    waitv<12>(); read_r(5);
    if (lane == 0) *(volatile LDS unsigned*)(size_t)(L_SYNC + 16 + l * 4) = 1;
    issue_r(7); issue_z(6);
    waitv<12>(); read_r(6); issue_r(8);
    waitv<8>(); read_r(7); issue_z(7);
    waitv<4>(); read_r(8); issue_z(8);
    waitv<0>();
    if (lane == 0) *(volatile LDS unsigned*)(size_t)(L_SYNC + 32 + l * 4) = 1;
    STAMP(3); STAMP(4); STAMP(5);
    {
      const u64 mytag = *(const LDS u64*)(size_t)(L_TAG + 63 * 8);
      const int b0 = 4 * rt2, b1 = b0 + 3;
      const int pl = 3 * (b0 >> 5) + (b0 & 31) / 11, ph = 3 * (b1 >> 5) + (b1 & 31) / 11;
      const int par = lane >= 14 ? 1 : 0, sl = lane - 14 * par;
      const bool need = lane < 28 && (par == (pl & 1) || par == (ph & 1)) && sl >= (9 * l) / 4 && sl <= (9 * l + 8) / 4;
      poll_flags(flags + bid * 64 + lane, need, mytag);
    }
    STAMP(6);
    f32x4 acc[4][4];
#pragma unroll
    for (int a = 0; a < 4; a++)
#pragma unroll
      for (int cb = 0; cb < 4; cb++) acc[a][cb] = f32x4{0.f, 0.f, 0.f, 0.f};
    {
      const unsigned abase = (rt2 * 72 + 16 + 9 * l) * 4096;
      constexpr int D = 8;
      i32x4 af[36];
#pragma unroll
      for (int n = 0; n < D; n++) af[n] = __builtin_amdgcn_raw_buffer_load_b128(ra, lane * 16 + (n & 3) * 1024, abase + (n >> 2) * 4096, 16);
#pragma unroll
      for (int n = 0; n < 36; n++) {
#pragma unroll
        for (int cb = 0; cb < 4; cb++) acc[n & 3][cb] = mfma32(wr[n >> 2][cb], __builtin_bit_cast(s16x8, af[n]), acc[n & 3][cb]);
        if (n + D < 36) af[n + D] = __builtin_amdgcn_raw_buffer_load_b128(ra, lane * 16 + ((n + D) & 3) * 1024, abase + ((n + D) >> 2) * 4096, 16);
        SB();
      }
    }
    STAMP(7);
    lds_spin4(L_SYNC + 48);
    {
      const unsigned pb = zpage(l == 0 ? 0 : l == 1 ? 1 : l == 2 ? 2 : 3, 0) * 4096;
#pragma unroll
      for (int a = 0; a < 4; a++)
#pragma unroll
        for (int cb = 0; cb < 4; cb++) *(LDS f32x4*)(size_t)(pb + (a * 4 + cb) * 1024 + lane * 16) = acc[a][cb];
    }
    waitl(); bar();
  } else {
    __builtin_amdgcn_s_setprio(1);
    if (w == 0) {
      const int lane = fresh_lane();
      int rt = (rb0 >> 2) + (lane >> 3), ct = lane & 7;
      bool pub = lane < 32 && rt <= ((rb0 + nblk - 1) >> 2);
      int cons = pub ? ((rt >> 2) + 8 * ((rt & 3) + 4 * ct)) : bid;
      u64 tg = __hip_atomic_load(epoch + cons * 8, __ATOMIC_RELAXED, __HIP_MEMORY_SCOPE_AGENT) + 1;
      *(LDS u64*)(size_t)(L_TAG + lane * 8) = tg;
      waitl();
    }
    if (is_mlp) {
      if (nblk == 11) mlp_path<11>(p, w, rb0, S TSPASS);
      else mlp_path<10>(p, w, rb0, S TSPASS);
    } else qkv_path(p, w, P, S TSPASS);
    STAMP(3);
    const int lane = fresh_lane(), i = lane & 15, q = lane >> 4;
    W2_OFFS();
    const u64 tg = *(const LDS u64*)(size_t)(L_TAG + lane * 8);
    const u64 mytag = *(const LDS u64*)(size_t)(L_TAG + 63 * 8);
    waitv<0>();
    unsigned old = 0;
    if (lane == 0) old = __hip_atomic_fetch_add((LDS unsigned*)(size_t)L_SYNC, 1u, __ATOMIC_RELAXED, __HIP_MEMORY_SCOPE_WORKGROUP);
    if (__builtin_amdgcn_readfirstlane(old) == 3) {
      int rt = (rb0 >> 2) + (lane >> 3), ct = lane & 7;
      if (lane < 32 && rt <= ((rb0 + nblk - 1) >> 2)) {
        int cons = (rt >> 2) + 8 * ((rt & 3) + 4 * ct);
        int slot = is_mlp ? ((P & 1) * 14 + S) : (28 + S);
        __hip_atomic_store(flags + cons * 64 + slot, tg, __ATOMIC_RELAXED, __HIP_MEMORY_SCOPE_AGENT);
      }
    }
    STAMP(4);
    const int otok = tok_of(4 * rt2 + w, i);
    u32x2 xres[4];
#pragma unroll
    for (int cb = 0; cb < 4; cb++) xres[cb] = *(const u32x2*)(p.x + otok * 1024 + ct2 * 128 + cb * 32 + q * 8);
    f32x4 as[4], ad[4];
#pragma unroll
    for (int cb = 0; cb < 4; cb++) { as[cb] = f32x4{0.f, 0.f, 0.f, 0.f}; ad[cb] = f32x4{0.f, 0.f, 0.f, 0.f}; }
    const unsigned abase = (rt2 * 72 * 4 + w) * 1024;
    poll_flags(flags + bid * 64 + lane, lane >= 28 && lane < 36, mytag);
    {
      i32x4 af[16];
#pragma unroll
      for (int n = 0; n < 16; n++) af[n] = __builtin_amdgcn_raw_buffer_load_b128(ra, lane * 16, abase + n * 4096, 16);
      lds_spin4(L_SYNC + 16);
#pragma unroll
      for (int n = 0; n < 16; n++) {
        const unsigned pg = zpage(n >> 2, n & 3) * 4096;
#pragma unroll
        for (int cb = 0; cb < 4; cb++) as[cb] = mfma32(lds_tr(pg + wb[cb], pg + wb[cb] + 2048), __builtin_bit_cast(s16x8, af[n]), as[cb]);
      }
    }
    waitl();
    if (lane == 0) *(volatile LDS unsigned*)(size_t)(L_SYNC + 48 + w * 4) = 1;
    STAMP(5);
    {
      const int b0 = 4 * rt2 + w;
      const int pl = 3 * (b0 >> 5) + (b0 & 31) / 11;
      const int lo = (pl & 1) * 14 + 9;
      poll_flags(flags + bid * 64 + lane, lane >= lo && lane < lo + 5, mytag);
    }
    STAMP(6);
    {
      i32x4 af[20];
#pragma unroll
      for (int n = 0; n < 20; n++) af[n] = __builtin_amdgcn_raw_buffer_load_b128(ra, lane * 16, abase + (52 + n) * 4096, 16);
      lds_spin4(L_SYNC + 32);
#pragma unroll
      for (int n = 0; n < 20; n++) {
        const unsigned pg = zpage(4 + (n >> 2), n & 3) * 4096;
#pragma unroll
        for (int cb = 0; cb < 4; cb++) ad[cb] = mfma32(lds_tr(pg + wb[cb], pg + wb[cb] + 2048), __builtin_bit_cast(s16x8, af[n]), ad[cb]);
      }
    }
    STAMP(7);
    bar();
    if (w == 0 && lane == 0) __hip_atomic_store(epoch + bid * 8, mytag, __ATOMIC_RELAXED, __HIP_MEMORY_SCOPE_AGENT);
#pragma unroll
    for (int cb = 0; cb < 4; cb++)
#pragma unroll
      for (int l = 0; l < 4; l++) ad[cb] += ldsf4(zpage(l, 0) * 4096 + (w * 4 + cb) * 1024 + lane * 16);
    unsigned o[4][2];
#pragma unroll
    for (int cb = 0; cb < 4; cb++)
#pragma unroll
      for (int h = 0; h < 2; h++) {
        f32x2 d = h ? hi2(ad[cb]) : lo2(ad[cb]), s = h ? hi2(as[cb]) : lo2(as[cb]);
        f32x2 e = d * sp2(-LOG2E);
        e[0] = __builtin_amdgcn_exp2f(e[0]); e[1] = __builtin_amdgcn_exp2f(e[1]);
        e = e + sp2(1.f);
        e[0] = __builtin_amdgcn_rcpf(e[0]); e[1] = __builtin_amdgcn_rcpf(e[1]);
        f32x2 xv = {__uint_as_float(xres[cb][h] << 16), __uint_as_float(xres[cb][h] & 0xffff0000u)};
        o[cb][h] = pack2(fma2(s * d, e, xv));
      }
#pragma unroll
    for (int c2 = 0; c2 < 2; c2++) {
      auto a = __builtin_amdgcn_permlane16_swap(o[2 * c2][0], o[2 * c2 + 1][0], false, false);
      auto b = __builtin_amdgcn_permlane16_swap(o[2 * c2][1], o[2 * c2 + 1][1], false, false);
      u32x4 v = {a[0], b[0], a[1], b[1]};
      *(u32x4*)(p.out + otok * 1024 + ct2 * 128 + c2 * 64 + (q & 1) * 32 + (q >> 1) * 16) = v;
    }
  }
  STAMP(8);
  if (w < 4) { u64 end_ = __builtin_amdgcn_s_memrealtime() + 10 * (ts[0] - ts[0]);
    while (__builtin_amdgcn_s_memrealtime() < end_) __builtin_amdgcn_s_sleep(2); }
  asm volatile("" :: "s"(cyc0));

}

extern "C" void kernel_launch(void* const* d_in, const int* in_sizes, int n_in, void* d_out, int out_size,
                              void* d_ws, size_t ws_size, hipStream_t stream) {
  Params p;
  p.x = (const char*)d_in[0]; p.wqkv = (const char*)d_in[1]; p.wo = (const char*)d_in[2];
  p.wg = (const char*)d_in[4]; p.wu = (const char*)d_in[5]; p.wd = (const char*)d_in[6];
  p.out = (char*)d_out; p.ws = (char*)d_ws;
  fused_kernel<<<256, 512, 0, stream>>>(p);
}
```

```cpp
#include <hip/hip_runtime.h>
#include <hip/hip_bf16.h>

typedef short s16x4 __attribute__((ext_vector_type(4)));
typedef short s16x8 __attribute__((ext_vector_type(8)));
typedef __bf16 bf2 __attribute__((ext_vector_type(2)));
typedef float f32x2 __attribute__((ext_vector_type(2)));
typedef float f32x4 __attribute__((ext_vector_type(4)));
typedef int i32x4 __attribute__((ext_vector_type(4)));
typedef unsigned u32x2 __attribute__((ext_vector_type(2)));
typedef unsigned u32x4 __attribute__((ext_vector_type(4)));
typedef unsigned long long u64;
#define LDS __attribute__((address_space(3)))
#define GLB __attribute__((address_space(1)))
#define DEV __device__ __forceinline__
#define SB() __builtin_amdgcn_sched_barrier(0)

#define ROTG 1

constexpr int L_W = 0;
constexpr int L_X = 65536;
constexpr int XSLOT = 24576;
constexpr int L_HALO = 139264;
constexpr int L_MISC = 155648;
constexpr int L_STAT = L_MISC;
constexpr int L_CS = L_MISC + 2560;
constexpr int L_TAG = L_MISC + 3584;
constexpr int L_SYNC = L_MISC + 4096;
constexpr int LDS_BYTES = 163840;

constexpr size_t WS_A = 0;
constexpr size_t WS_A_BYTES = 32u * 72u * 4096u;
constexpr size_t WS_FLAG = WS_A_BYTES;
constexpr size_t WS_EPOCH = WS_FLAG + 256 * 512;
constexpr size_t WS_STAMP = 32u << 20;

#define IS_MLP_R(r) ((r) < 21)
struct Params {
  const char* x; const char* wqkv; const char* wo; const char* wg; const char* wu; const char* wd;
  char* out; char* ws;
};

DEV i32x4 make_srd(const void* p, unsigned bytes) {
  u64 a = (u64)p;
  i32x4 r; r[0] = (int)(unsigned)a; r[1] = (int)((unsigned)(a >> 32) & 0xffffu); r[2] = (int)bytes; r[3] = 0x00020000;
  return r;
}
DEV void dma16(unsigned lds, unsigned voff, i32x4 srd, unsigned soff) {
  asm volatile("s_mov_b32 m0, %0\n\ts_nop 0\n\tbuffer_load_dwordx4 %1, %2, %3 offen lds"
               :: "s"(lds), "v"(voff), "s"(srd), "s"(soff) : "memory");
}
DEV void dma16m(unsigned lds, unsigned voff, i32x4 srd, unsigned soff, u64 mask) {
  asm volatile("s_mov_b32 m0, %0\n\ts_mov_b64 exec, %4\n\tbuffer_load_dwordx4 %1, %2, %3 offen lds\n\ts_mov_b64 exec, -1"
               :: "s"(lds), "v"(voff), "s"(srd), "s"(soff), "s"(mask) : "memory");
}
DEV void store16_sc1(i32x4 v, unsigned voff, i32x4 srd, unsigned soff) {
  asm volatile("buffer_store_dwordx4 %0, %1, %2, %3 offen sc1\n\ts_nop 1" :: "v"(v), "v"(voff), "s"(srd), "s"(soff) : "memory");
}
template <int N> DEV void waitv() { asm volatile("s_waitcnt vmcnt(%0)" :: "n"(N) : "memory"); }
DEV void waitl() { __builtin_amdgcn_s_waitcnt(0xC07F); asm volatile("" ::: "memory"); }
DEV void bar() { asm volatile("s_barrier" ::: "memory"); }
DEV s16x8 lds_b128(unsigned a) { return *(const LDS s16x8*)(size_t)a; }
DEV s16x4 lds_tr4(unsigned a) { return __builtin_amdgcn_ds_read_tr16_b64_v4i16((LDS s16x4*)(size_t)a); }
DEV s16x8 cat(s16x4 lo, s16x4 hi) { return __builtin_shufflevector(lo, hi, 0, 1, 2, 3, 4, 5, 6, 7); }
DEV s16x8 lds_tr(unsigned a0, unsigned a1) { return cat(lds_tr4(a0), lds_tr4(a1)); }
DEV f32x4 ldsf4(unsigned a) { return *(const LDS f32x4*)(size_t)a; }
DEV float ldsf(unsigned a) { return *(const LDS float*)(size_t)a; }
DEV f32x4 mfma32(s16x8 a, s16x8 b, f32x4 c) { return __builtin_amdgcn_mfma_f32_16x16x32_bf16(a, b, c, 0, 0, 0); }
DEV f32x4 mfma16(u32x2 a, u32x2 b, f32x4 c) {
  return __builtin_amdgcn_mfma_f32_16x16x16bf16_1k(__builtin_bit_cast(s16x4, a), __builtin_bit_cast(s16x4, b), c, 0, 0, 0);
}
DEV float dot2(unsigned a, unsigned b, float c) {
  return __builtin_amdgcn_fdot2_f32_bf16(__builtin_bit_cast(bf2, a), __builtin_bit_cast(bf2, b), c, false);
}
DEV unsigned pack2(float a, float b) { bf2 p = {(__bf16)a, (__bf16)b}; return __builtin_bit_cast(unsigned, p); }
DEV unsigned pack2(f32x2 a) { return pack2(a[0], a[1]); }
DEV float rbf(float a) { return (float)(__bf16)a; }
DEV f32x2 lo2(f32x4 a) { return f32x2{a[0], a[1]}; }
DEV f32x2 hi2(f32x4 a) { return f32x2{a[2], a[3]}; }
DEV f32x2 fma2(f32x2 a, f32x2 b, f32x2 c) { return __builtin_elementwise_fma(a, b, c); }
DEV f32x2 sp2(float a) { return f32x2{a, a}; }
DEV float red4(float v0, float v1, float v2, float v3) {
  auto a = __builtin_amdgcn_permlane32_swap(__float_as_uint(v0), __float_as_uint(v2), false, false);
  auto b = __builtin_amdgcn_permlane32_swap(__float_as_uint(v1), __float_as_uint(v3), false, false);
  float s02 = __uint_as_float(a[0]) + __uint_as_float(a[1]);
  float s13 = __uint_as_float(b[0]) + __uint_as_float(b[1]);
  auto c = __builtin_amdgcn_permlane16_swap(__float_as_uint(s02), __float_as_uint(s13), false, false);
  return __uint_as_float(c[0]) + __uint_as_float(c[1]);
}
DEV float allq_max(float v) {
  auto a = __builtin_amdgcn_permlane32_swap(__float_as_uint(v), __float_as_uint(v), false, false);
  v = fmaxf(__uint_as_float(a[0]), __uint_as_float(a[1]));
  auto b = __builtin_amdgcn_permlane16_swap(__float_as_uint(v), __float_as_uint(v), false, false);
  return fmaxf(__uint_as_float(b[0]), __uint_as_float(b[1]));
}
DEV int fresh_lane() { int l = __builtin_amdgcn_mbcnt_hi(~0u, __builtin_amdgcn_mbcnt_lo(~0u, 0u)); asm volatile("" : "+v"(l)); return l; }
DEV int tok_of(int rb, int i) { return 32 * (rb >> 1) + 2 * i + (rb & 1); }
DEV void lds_spin4(unsigned a) {
  for (;;) {
    u32x4 v = *(volatile LDS u32x4*)(size_t)a;
    if (__builtin_amdgcn_readfirstlane(v[0] & v[1] & v[2] & v[3])) break;
    __builtin_amdgcn_s_sleep(1);
  }
  asm volatile("" ::: "memory");
}
DEV void poll_flags(GLB u64* fl, bool need, u64 tag) {
  unsigned spins = 0;
  for (;;) {
    u64 v = __hip_atomic_load(fl, __ATOMIC_RELAXED, __HIP_MEMORY_SCOPE_AGENT);
    if (__all(!need || v == tag)) break;
    if (++spins > (1u << 20)) break;
    __builtin_amdgcn_s_sleep(1);
  }
  __builtin_amdgcn_fence(__ATOMIC_ACQUIRE, "wavefront");
  asm volatile("" ::: "memory");
}

#define STAMP(n)
#define TSARG
#define TSPASS

constexpr float LOG2E = 1.4426950408889634f;

template <bool QKV, int NB, int NC>
struct S1 { f32x4 acc[NB][NC]; float cs[NC]; };

template <bool QKV> DEV s16x4 w_half(int idx, unsigned wa0, unsigned wa1) {
  const int c = idx >> 1, t = idx & 1;
  if (QKV) return lds_tr4(wa0 + c * 128 + t * 4 * 384);
  return lds_tr4(((c & 1) ? wa1 : wa0) + (c >> 1) * 8192 + t * 1024);
}
template <bool QKV> DEV s16x8 x_frag(int b, unsigned xa, unsigned ha) {
  if (QKV) return b == 0 ? lds_b128(ha) : lds_b128(xa + (b - 1) * 2048);
  return lds_b128(xa + b * 2048);
}
constexpr int PF = 3;
template <bool QKV, int NB, int NC, bool LAST>
DEV void c_step(S1<QKV, NB, NC>& st, s16x8 (&wf)[NC], s16x8 (&xp)[PF],
                unsigned xa, unsigned ha, unsigned xan, unsigned han, unsigned wn0, unsigned wn1) {
  s16x8 xf[NB + PF]; s16x4 wh[2 * NC];
#pragma unroll
  for (int j = 0; j < PF; j++) xf[j] = xp[j];
#pragma unroll
  for (int b = 0; b < NB; b++) {
    if (!LAST && b < NC) { wh[2 * b] = w_half<QKV>(2 * b, wn0, wn1); wh[2 * b + 1] = w_half<QKV>(2 * b + 1, wn0, wn1); }
    if (b + PF < NB) xf[b + PF] = x_frag<QKV>(b + PF, xa, ha);
    else if (!LAST) xf[b + PF] = x_frag<QKV>(b + PF - NB, xan, han);
#pragma unroll
    for (int c = 0; c < NC; c++) {
      if (QKV && b == 0 && c == 0) continue;
      if (QKV && c == 2) st.acc[b][c] = mfma32(xf[b], wf[c], st.acc[b][c]);
      else st.acc[b][c] = mfma32(wf[c], xf[b], st.acc[b][c]);
    }
    if (b < 2 * NC) {
      u32x4 u = __builtin_bit_cast(u32x4, wf[b >> 1]);
      st.cs[b >> 1] = dot2(u[2 * (b & 1)], 0x3f803f80u, st.cs[b >> 1]);
      st.cs[b >> 1] = dot2(u[2 * (b & 1) + 1], 0x3f803f80u, st.cs[b >> 1]);
    }
    SB();
  }
  if (!LAST) {
#pragma unroll
    for (int c = 0; c < NC; c++) wf[c] = cat(wh[2 * c], wh[2 * c + 1]);
#pragma unroll
    for (int j = 0; j < PF; j++) xp[j] = xf[NB + j];
  }
}
template <bool QKV, int NB, int NC>
DEV void c_main(S1<QKV, NB, NC>& st, unsigned xa, unsigned wa0, unsigned wa1 TSARG) {
#pragma unroll
  for (int b = 0; b < NB; b++)
#pragma unroll
    for (int c = 0; c < NC; c++) st.acc[b][c] = f32x4{0.f, 0.f, 0.f, 0.f};
#pragma unroll
  for (int c = 0; c < NC; c++) st.cs[c] = 0.f;
  s16x8 wf[NC], xp[PF];
  bar();
  STAMP(1);
#pragma unroll
  for (int c = 0; c < NC; c++) wf[c] = cat(w_half<QKV>(2 * c, L_W + wa0, L_W + wa1), w_half<QKV>(2 * c + 1, L_W + wa0, L_W + wa1));
#pragma unroll
  for (int j = 0; j < PF; j++) xp[j] = x_frag<QKV>(j, L_X + xa, L_HALO + xa);
  waitl();
  unsigned xb = L_X, hb = L_HALO, wb = 0;
#pragma unroll 1
  for (int it = 0; it < 8; it++) {
    unsigned xbn = (xb == L_X + 2 * XSLOT) ? L_X : xb + XSLOT;
    bar();
    c_step<QKV, NB, NC, false>(st, wf, xp, xb + xa, hb + xa, xb + (xa ^ 64), hb + (xa ^ 64), wb + 16384 + wa0, wb + 16384 + wa1);
    wb ^= 32768;
    bar();
    c_step<QKV, NB, NC, false>(st, wf, xp, xb + (xa ^ 64), hb + (xa ^ 64), xbn + xa, hb + 2048 + xa, wb + wa0, wb + wa1);
    xb = xbn; hb += 2048;
  }
}

template <int NB>
DEV void mlp_path(const Params& p, int w, int rb0, int S TSARG) {
  int lane = fresh_lane(), i = lane & 15, q = lane >> 4;
  S1<false, NB, 4> st;
  const unsigned xa = i * 128 + ((q ^ (i >> 1)) & 7) * 16;
  const int krow = 8 * q + (i >> 2);
  const int f = (krow & 3) | (((krow >> 3) & 1) << 2);
  const unsigned wa0 = krow * 256 + (((2 * w) ^ f) & 7) * 32 + (i & 3) * 8;
  c_main<false, NB, 4>(st, xa, wa0, wa0 ^ 32 TSPASS);
  lane = fresh_lane(); i = lane & 15; q = lane >> 4;
  *(LDS float*)(size_t)(L_CS + w * 256 + lane * 4) = red4(st.cs[0], st.cs[1], st.cs[2], st.cs[3]);
  bar();
  STAMP(2);
  f32x4 cs[4];
#pragma unroll
  for (int c = 0; c < 4; c++) cs[c] = ldsf4(L_CS + w * 256 + c * 64 + q * 16);
  float nm[NB + 1], sd[NB + 1], c1[NB + 1];
  nm[0] = ldsf(L_STAT + i * 4); sd[0] = ldsf(L_STAT + 64 + i * 4); c1[0] = ldsf(L_STAT + 128 + i * 4);
  const i32x4 sa = make_srd(p.ws + WS_A, (unsigned)WS_A_BYTES);
  const int ks = 16 + 4 * S + w;
#pragma unroll
  for (int b = 0; b < NB; b++) {
    f32x2 z[4], u[4];
    if (b + 1 < NB) { nm[b + 1] = ldsf(L_STAT + (b + 1) * 192 + i * 4); sd[b + 1] = ldsf(L_STAT + (b + 1) * 192 + 64 + i * 4); c1[b + 1] = ldsf(L_STAT + (b + 1) * 192 + 128 + i * 4); }
#pragma unroll
    for (int j = 0; j < 4; j++) {
      f32x2 a = (j & 1) ? hi2(st.acc[b][j >> 1]) : lo2(st.acc[b][j >> 1]);
      f32x2 c = (j & 1) ? hi2(cs[j >> 1]) : lo2(cs[j >> 1]);
      z[j] = fma2(c, sp2(nm[b]), a);
    }
    SB();
#pragma unroll
    for (int j = 0; j < 4; j++) z[j] = z[j] * sp2(c1[b]);
    SB();
#pragma unroll
    for (int j = 0; j < 4; j++) {
      z[j][0] = __builtin_amdgcn_exp2f(z[j][0]); z[j][1] = __builtin_amdgcn_exp2f(z[j][1]);
      f32x2 a = (j & 1) ? hi2(st.acc[b][2 + (j >> 1)]) : lo2(st.acc[b][2 + (j >> 1)]);
      f32x2 c = (j & 1) ? hi2(cs[2 + (j >> 1)]) : lo2(cs[2 + (j >> 1)]);
      u[j] = fma2(c, sp2(nm[b]), a);
    }
    SB();
#pragma unroll
    for (int j = 0; j < 4; j++) z[j] = fma2(z[j], sp2(sd[b]), sp2(sd[b]));
    SB();
#pragma unroll
    for (int j = 0; j < 4; j++) { z[j][0] = __builtin_amdgcn_rcpf(z[j][0]); z[j][1] = __builtin_amdgcn_rcpf(z[j][1]); }
    SB();
#pragma unroll
    for (int j = 0; j < 4; j++) u[j] = u[j] * z[j];
    SB();
    i32x4 v = {(int)pack2(u[0]), (int)pack2(u[1]), (int)pack2(u[2]), (int)pack2(u[3])};
    const int rb = rb0 + b;
    store16_sc1(v, lane * 16, sa, (((rb >> 2) * 72 + ks) * 4 + (rb & 3)) * 1024);
  }
}

DEV void qkv_path(const Params& p, int w, int P, int G TSARG) {
  int lane = fresh_lane(), i = lane & 15, q = lane >> 4;
  S1<true, 13, 3> st;
  const unsigned xa = i * 128 + ((q ^ (i >> 1)) & 7) * 16;
  const int krow = 8 * q + (i >> 2);
  const int f = ((krow >> 1) & 1) | (((krow >> 3) & 1) << 1);
  const unsigned wa0 = krow * 384 + ((w ^ f) & 3) * 32 + (i & 3) * 8;
  c_main<true, 13, 3>(st, xa, wa0, wa0 TSPASS);
  lane = fresh_lane(); i = lane & 15; q = lane >> 4;
  *(LDS float*)(size_t)(L_CS + w * 256 + lane * 4) = red4(st.cs[0], st.cs[1], st.cs[2], st.cs[2]);
  bar();
  STAMP(2);
  const f32x4 csq = ldsf4(L_CS + w * 256 + q * 16), csk = ldsf4(L_CS + w * 256 + 64 + q * 16);
  const float ncsv = ldsf(L_CS + w * 256 + 128 + i * 4);
  u32x2 Q[13], K[13], V[13]; float qs[13];
  auto fold = [&](int b) {
    const int tb = L_STAT + (b == 0 ? 12 : b - 1) * 192;
    const float nm = ldsf(tb + i * 4), rstd = ldsf(tb + 128 + i * 4);
    const f32x4 m4 = ldsf4(tb + q * 16), r4 = ldsf4(tb + 128 + q * 16);
    if (b > 0) {
      Q[b] = u32x2{pack2(fma2(lo2(csq), sp2(nm), lo2(st.acc[b][0]))), pack2(fma2(hi2(csq), sp2(nm), hi2(st.acc[b][0])))};
      qs[b] = rstd * (0.25f * LOG2E);
    }
    K[b] = u32x2{pack2(fma2(lo2(csk), sp2(nm), lo2(st.acc[b][1])) * sp2(rstd)), pack2(fma2(hi2(csk), sp2(nm), hi2(st.acc[b][1])) * sp2(rstd))};
    V[b] = u32x2{pack2(fma2(lo2(m4), sp2(ncsv), lo2(st.acc[b][2])) * lo2(r4)), pack2(fma2(hi2(m4), sp2(ncsv), hi2(st.acc[b][2])) * hi2(r4))};
  };
  fold(0);
  const float NEG = -__builtin_inff();
  f32x4 mcur, mprev, mh0, mh1; u32x2 selc;
  const bool first = (P == 0);
#pragma unroll
  for (int r = 0; r < 4; r++) {
    int kap = 4 * q + r;
    mcur[r] = (kap <= i && kap >= i - 7) ? 0.f : NEG;
    mprev[r] = (kap >= i + 9) ? 0.f : NEG;
    mh0[r] = (!first && kap < 8 && kap >= i + 1) ? 0.f : NEG;
    mh1[r] = (!first && kap >= i + 9) ? 0.f : NEG;
  }
  selc[0] = ((4 * q <= i) ? 0xffffu : 0u) | ((4 * q + 1 <= i) ? 0xffff0000u : 0u);
  selc[1] = ((4 * q + 2 <= i) ? 0xffffu : 0u) | ((4 * q + 3 <= i) ? 0xffff0000u : 0u);
  const i32x4 sa = make_srd(p.ws + WS_A, (unsigned)WS_A_BYTES);
  const int head = 4 * G + w;
  const unsigned voff = (q & 1) * 1024 + ((2 * (head & 1) + (q >> 1)) * 16 + i) * 16;
  const u32x2 ones = {0x3f803f80u, 0x3f803f80u};
  const f32x4 zero = {0.f, 0.f, 0.f, 0.f};
#pragma unroll
  for (int g2 = 0; g2 < 3; g2++) {
    f32x4 sc[4], sp[4]; float mx[4]; u32x2 pk[4]; f32x4 o[4], sm[4];
#pragma unroll
    for (int u = 0; u < 4; u++) fold(1 + 4 * g2 + u);
#pragma unroll
    for (int u = 0; u < 4; u++) {
      const int b = 1 + 4 * g2 + u, pb = (b <= 2) ? 0 : b - 2;
      sc[u] = mfma16(K[b], Q[b], mcur);
      sp[u] = mfma16(K[pb], Q[b], b == 1 ? mh0 : (b == 2 ? mh1 : mprev));
    }
#pragma unroll
    for (int u = 0; u < 4; u++) {
#pragma unroll
      for (int r = 0; r < 4; r++) sc[u][r] = fmaxf(sc[u][r], sp[u][r]);
      mx[u] = fmaxf(fmaxf(sc[u][0], sc[u][1]), fmaxf(sc[u][2], sc[u][3]));
    }
#pragma unroll
    for (int u = 0; u < 4; u++) mx[u] = allq_max(mx[u]);
#pragma unroll
    for (int u = 0; u < 4; u++) {
      const int b = 1 + 4 * g2 + u;
      const float nm = -mx[u] * qs[b];
      f32x2 a = fma2(lo2(sc[u]), sp2(qs[b]), sp2(nm)), c = fma2(hi2(sc[u]), sp2(qs[b]), sp2(nm));
      pk[u] = u32x2{pack2(__builtin_amdgcn_exp2f(a[0]), __builtin_amdgcn_exp2f(a[1])), pack2(__builtin_amdgcn_exp2f(c[0]), __builtin_amdgcn_exp2f(c[1]))};
    }
#pragma unroll
    for (int u = 0; u < 4; u++) {
      const int b = 1 + 4 * g2 + u, pb = (b <= 2) ? 0 : b - 2;
      u32x4 pf = {pk[u][0] & ~selc[0], pk[u][1] & ~selc[1], pk[u][0] & selc[0], pk[u][1] & selc[1]};
      u32x4 vf = {V[pb][0], V[pb][1], V[b][0], V[b][1]};
      sm[u] = mfma16(ones, pk[u], zero);
      o[u] = mfma32(__builtin_bit_cast(s16x8, vf), __builtin_bit_cast(s16x8, pf), zero);
    }
#pragma unroll
    for (int g = 0; g < 2; g++) {
      u32x2 O[2];
#pragma unroll
      for (int par = 0; par < 2; par++) {
        const float inv = __builtin_amdgcn_rcpf(sm[2 * g + par][0]);
        O[par] = u32x2{pack2(lo2(o[2 * g + par]) * sp2(inv)), pack2(hi2(o[2 * g + par]) * sp2(inv))};
      }
      auto a = __builtin_amdgcn_permlane16_swap(O[0][0], O[1][0], false, false);
      auto b2 = __builtin_amdgcn_permlane16_swap(O[0][1], O[1][1], false, false);
      i32x4 v = {(int)a[0], (int)b2[0], (int)a[1], (int)b2[1]};
      const int rb = 12 * P + 4 * g2 + 2 * g;
      if (rb < 128) store16_sc1(v, voff, sa, (((rb >> 2) * 72 + (head >> 1)) * 4 + (rb & 3)) * 1024);
    }
  }
}

DEV constexpr int zpage(int j, int l) {
  return j == 0 ? 4 + l : j == 1 ? 8 + l : j == 2 ? 16 + l : j == 3 ? 12 + l : j == 4 ? (l < 2 ? 20 + l : 30 + l)
       : j == 5 ? 22 + l : j == 6 ? 34 + l : j == 7 ? 28 + l : 0 + l;
}

__global__ __launch_bounds__(512, 2) void fused_kernel(Params p) {
  __shared__ __attribute__((aligned(16))) char smem[LDS_BYTES];
  asm volatile("" :: "s"((unsigned)(size_t)(LDS char*)smem));
  const int w = __builtin_amdgcn_readfirstlane(threadIdx.x >> 6);
  const int bid = blockIdx.x, xcd = bid & 7, r = bid >> 3;
  STAMP(0);
  const bool is_mlp = IS_MLP_R(r);
  int P, S, rb0, nblk;
  if (is_mlp) { int idx = (xcd >> 1) * 21 + r; S = idx / 6; P = 6 * (xcd & 1) + idx % 6; rb0 = 32 * (P / 3) + 11 * (P % 3); nblk = (P % 3 == 2) ? 10 : 11; }
  else { int j = (xcd & 1) * 11 + (r - 21); P = j >> 1; S = 2 * (xcd >> 1) + (j & 1); rb0 = 12 * P; nblk = (P == 10) ? 8 : 12; }
  const int rt2 = 4 * xcd + (r & 3), ct2 = r >> 2;
  GLB u64* epoch = (GLB u64*)(p.ws + WS_EPOCH);
  GLB u64* flags = (GLB u64*)(p.ws + WS_FLAG);
  __amdgpu_buffer_rsrc_t ra = __builtin_amdgcn_make_buffer_rsrc(p.ws + WS_A, 0, (int)WS_A_BYTES, 0x00020000);
#define W2_OFFS() unsigned wb[4]; { const int krow = 4 * q + (i >> 2); \
    _Pragma("unroll") for (int cb = 0; cb < 4; cb++) wb[cb] = krow * 128 + ((cb ^ (krow >> 1)) & 3) * 32 + (i & 3) * 8; }

  if (w >= 4) {
    const int l = w - 4;
    const int lane = fresh_lane(), i = lane & 15, q = lane >> 4;
    W2_OFFS();
    const int rot = ROTG == 1 ? 0 : (((P + S) % ROTG) * (8 / ROTG));
    const i32x4 srd_x = make_srd(p.x, 2048 * 1024);
    const int RA = (is_mlp && nblk == 10) ? 20 : 24, RB = is_mlp ? 20 : 24;
    unsigned vx[2][3], lx[2];
    const u64 mxa = RA == 20 ? 0xffffffffull : ~0ull, mxb = RB == 20 ? 0xffffffffull : ~0ull;
#pragma unroll
    for (int h = 0; h < 2; h++) {
      const int row0 = h ? 4 * RA + l * RB : l * RA;
      lx[h] = row0 * 128;
#pragma unroll
      for (int j = 0; j < 3; j++) {
        int rho = row0 + 8 * j + (lane >> 3);
        int rb = rb0 + (rho >> 4); if (rb > 127) rb = 127;
        vx[h][j] = tok_of(rb, rho & 15) * 1024 + (((lane & 7) ^ (rho >> 1)) & 7) * 16;
      }
    }
    i32x4 srd_w0, srd_w1; unsigned vw[2], lw0, lwj, lw2, swj, sw2, wpitch32; u64 mw;
    if (is_mlp) {
      srd_w0 = make_srd(p.wg, 512 * 3584); srd_w1 = make_srd(p.wu, 512 * 3584);
      const int f = q | ((l & 1) << 2);
      vw[0] = vw[1] = (8 * l + q) * 3584 + S * 256 + (((i >> 1) ^ f) & 7) * 32 + (i & 1) * 16;
      lw0 = l * 2048; lwj = 1024; lw2 = 8192; swj = 4 * 3584; sw2 = 0; wpitch32 = 32 * 3584; mw = ~0ull;
    } else {
      srd_w0 = srd_w1 = make_srd(p.wqkv, 512 * 3072);
      const int row = lane >= 24 ? 1 : 0, rem = lane - 24 * row;
#pragma unroll
      for (int j = 0; j < 2; j++) {
        const int f = j | ((l & 1) << 1);
        vw[j] = (8 * l + row) * 3072 + (rem >> 3) * 1024 + S * 128 + ((((rem & 7) >> 1) ^ f) & 3) * 32 + (rem & 1) * 16;
      }
      lw0 = l * 3072; lwj = 768; lw2 = 1536; swj = 2 * 3072; sw2 = 4 * 3072; wpitch32 = 32 * 3072; mw = 0xffffffffffffull;
#pragma unroll
      for (int j = 0; j < 2; j++) {
        const int rho = 8 * j + (lane >> 3);
        int tok = 192 * P - 16 + 2 * (rho & 7) + j; if (tok < 0) tok = 0;
        const unsigned v = tok * 1024 + (((lane & 7) ^ (rho >> 1)) & 7) * 16;
        dma16(L_HALO + l * 2048 + j * 1024, v, srd_x, ((l + rot) & 7) * 128);
        dma16(L_HALO + (l + 4) * 2048 + j * 1024, v, srd_x, ((l + 4 + rot) & 7) * 128);
      }
    }
    auto issue_w = [&](int s) {
      const unsigned base = L_W + (s & 3) * 16384 + lw0, so = ((s + 2 * rot) & 15) * wpitch32;
      dma16m(base, vw[0], srd_w0, so, mw);
      dma16m(base + lwj, vw[1], srd_w0, so + swj, mw);
      dma16m(base + lw2, vw[0], srd_w1, so + sw2, mw);
      dma16m(base + lw2 + lwj, vw[1], srd_w1, so + sw2 + swj, mw);
    };
    auto issue_x = [&](int c, unsigned slotbase, int h) {
      const unsigned base = slotbase + lx[h], so = ((c + rot) & 7) * 128;
      dma16(base, vx[h][0], srd_x, so);
      dma16(base + 1024, vx[h][1], srd_x, so);
      dma16m(base + 2048, vx[h][2], srd_x, so, h ? mxb : mxa);
    };
    issue_x(0, L_X, 0); issue_x(0, L_X, 1); issue_w(0); issue_w(1); issue_x(1, L_X + XSLOT, 0);
    const unsigned xa = i * 128 + ((q ^ (i >> 1)) & 7) * 16;
    const int lastb = is_mlp ? nblk - 1 : 11;
    const unsigned b2off = ((l + 8 > lastb ? lastb : l + 8) - l) * 2048;
    float rs[4] = {0.f, 0.f, 0.f, 0.f}, rq[4] = {0.f, 0.f, 0.f, 0.f};
    const bool do_halo = !is_mlp && l == 0;
    u32x4 sf[4];
    auto stat_read = [&](unsigned xb, unsigned hb) {
      sf[0] = __builtin_bit_cast(u32x4, lds_b128(xb + l * 2048));
      sf[1] = __builtin_bit_cast(u32x4, lds_b128(xb + l * 2048 + 8192));
      sf[2] = __builtin_bit_cast(u32x4, lds_b128(xb + l * 2048 + b2off));
      if (do_halo) sf[3] = __builtin_bit_cast(u32x4, lds_b128(hb));
    };
    auto stat_acc = [&]() {
#pragma unroll
      for (int j = 0; j < 4; j++)
#pragma unroll
        for (int b = 0; b < 3; b++) { rs[b] = dot2(sf[b][j], 0x3f803f80u, rs[b]); rq[b] = dot2(sf[b][j], sf[b][j], rq[b]); }
      if (do_halo) {
#pragma unroll
        for (int j = 0; j < 4; j++) { rs[3] = dot2(sf[3][j], 0x3f803f80u, rs[3]); rq[3] = dot2(sf[3][j], sf[3][j], rq[3]); }
      }
    };
    if (lane < 16) *(LDS unsigned*)(size_t)(L_SYNC + lane * 4) = 0;
    sf[3] = u32x4{0, 0, 0, 0};
    waitl();
    waitv<7>(); bar();
    STAMP(1);
    unsigned x0 = L_X, x1 = L_X + XSLOT, x2 = L_X + 2 * XSLOT, hb = L_HALO;
#pragma unroll 1
    for (int c = 0; c < 6; c++) {
      stat_read(x0 + xa, hb + xa);
      issue_x(c + 1, x1, 1); issue_w(2 * c + 2);
      stat_acc();
      waitv<10>(); bar();
      stat_read(x0 + (xa ^ 64), hb + (xa ^ 64));
      issue_w(2 * c + 3); issue_x(c + 2, x2, 0);
      stat_acc();
      waitv<7>(); bar();
      unsigned t = x0; x0 = x1; x1 = x2; x2 = t; hb += 2048;
    }
    const i32x4 srd_o = make_srd(p.wo, 512 * 1024), srd_d = make_srd(p.wd, 1792 * 1024);
    unsigned voff_o, voff_d;
    {
      const int rr = lane >> 3; const unsigned col = ct2 * 128 + ((((lane & 7) >> 1) ^ (rr >> 1)) & 3) * 32 + (lane & 1) * 16;
      voff_d = rr * 1024 + col;
      voff_o = (8 * (rr >> 2) + (rr & 3)) * 1024 + col;
    }
    auto page_d = [&](int page, int hks) {
#pragma unroll
      for (int o = 0; o < 4; o++) dma16(page * 4096 + o * 1024, voff_d, srd_d, (hks * 32 + 8 * o) * 1024);
    };
    auto page_o = [&](int page, int ks) {
#pragma unroll
      for (int o = 0; o < 4; o++) dma16(page * 4096 + o * 1024, voff_o, srd_o, (ks * 32 + 16 * (o & 1) + 4 * (o >> 1)) * 1024);
    };
    auto issue_z = [&](int j) { if (j < 4) page_o(zpage(j, l), 4 * j + l); else page_d(zpage(j, l), 36 + 4 * (j - 4) + l); };
    auto issue_r = [&](int n) { page_d((n & 1) ? 28 + l : l, 9 * l + n); };
    s16x8 wr[9][4];
    auto read_r = [&](int n) {
      const unsigned pg = ((n & 1) ? 28 + l : l) * 4096;
#pragma unroll
      for (int cb = 0; cb < 4; cb++) wr[n][cb] = lds_tr(pg + wb[cb], pg + wb[cb] + 2048);
      waitl();
    };
    stat_read(x0 + xa, hb + xa);
    issue_x(7, x1, 1); issue_w(14);
    stat_acc();
    waitv<10>(); bar();
    stat_read(x0 + (xa ^ 64), hb + (xa ^ 64));
    issue_w(15); issue_r(0);
    stat_acc();
    waitv<8>(); bar();
    stat_read(x1 + xa, hb + 2048 + xa);
    issue_r(1); issue_z(0);
    stat_acc();
    waitv<12>(); bar();
    stat_read(x1 + (xa ^ 64), hb + 2048 + (xa ^ 64));
    waitv<8>(); read_r(0); issue_r(2); issue_z(1);
    stat_acc();
    bar();
    {
      float s = red4(rs[0], rs[1], rs[2], rs[3]), t = red4(rq[0], rq[1], rq[2], rq[3]);
      float m = s * (1.f / 512.f);
      float var = fmaxf(t * (1.f / 512.f) - m * m, 0.f);
      float sd = rbf(__builtin_amdgcn_sqrtf(rbf(rbf(var) + 1e-5f)));
      int blk = q == 3 ? 12 : l + 4 * q;
      if (q == 3 ? do_halo : (blk <= lastb)) {
        *(LDS float*)(size_t)(L_STAT + blk * 192 + i * 4) = -rbf(m);
        *(LDS float*)(size_t)(L_STAT + blk * 192 + 64 + i * 4) = sd;
        *(LDS float*)(size_t)(L_STAT + blk * 192 + 128 + i * 4) = __builtin_amdgcn_rcpf(sd) * (is_mlp ? -LOG2E : 1.f);
      }
    }
    waitv<12>(); read_r(1); issue_r(3); issue_z(2);
    waitl(); bar();
    STAMP(2);
    waitv<12>(); read_r(2); issue_r(4); issue_z(3);
    waitv<12>(); read_r(3); issue_r(5); issue_z(4);
    waitv<12>(); read_r(4); issue_r(6); issue_z(5);
    waitv<12>(); read_r(5);
    if (lane == 0) *(volatile LDS unsigned*)(size_t)(L_SYNC + 16 + l * 4) = 1;
    issue_r(7); issue_z(6);
    waitv<12>(); read_r(6); issue_r(8);
    waitv<8>(); read_r(7); issue_z(7);
    waitv<4>(); read_r(8); issue_z(8);
    waitv<0>();
    if (lane == 0) *(volatile LDS unsigned*)(size_t)(L_SYNC + 32 + l * 4) = 1;
    STAMP(3); STAMP(4); STAMP(5);
    {
      const u64 mytag = *(const LDS u64*)(size_t)(L_TAG + 63 * 8);
      const int b0 = 4 * rt2, b1 = b0 + 3;
      const int pl = 3 * (b0 >> 5) + (b0 & 31) / 11, ph = 3 * (b1 >> 5) + (b1 & 31) / 11;
      const int par = lane >= 14 ? 1 : 0, sl = lane - 14 * par;
      const bool need = lane < 28 && (par == (pl & 1) || par == (ph & 1)) && sl >= (9 * l) / 4 && sl <= (9 * l + 8) / 4;
      poll_flags(flags + bid * 64 + lane, need, mytag);
    }
    STAMP(6);
    f32x4 acc[4][4];
#pragma unroll
    for (int a = 0; a < 4; a++)
#pragma unroll
      for (int cb = 0; cb < 4; cb++) acc[a][cb] = f32x4{0.f, 0.f, 0.f, 0.f};
    {
      const unsigned abase = (rt2 * 72 + 16 + 9 * l) * 4096;
      constexpr int D = 8;
      i32x4 af[36];
#pragma unroll
      for (int n = 0; n < D; n++) af[n] = __builtin_amdgcn_raw_buffer_load_b128(ra, lane * 16 + (n & 3) * 1024, abase + (n >> 2) * 4096, 16);
#pragma unroll
      for (int n = 0; n < 36; n++) {
#pragma unroll
        for (int cb = 0; cb < 4; cb++) acc[n & 3][cb] = mfma32(wr[n >> 2][cb], __builtin_bit_cast(s16x8, af[n]), acc[n & 3][cb]);
        if (n + D < 36) af[n + D] = __builtin_amdgcn_raw_buffer_load_b128(ra, lane * 16 + ((n + D) & 3) * 1024, abase + ((n + D) >> 2) * 4096, 16);
        SB();
      }
    }
    STAMP(7);
    lds_spin4(L_SYNC + 48);
    {
      const unsigned pb = zpage(l == 0 ? 0 : l == 1 ? 1 : l == 2 ? 2 : 3, 0) * 4096;
#pragma unroll
      for (int a = 0; a < 4; a++)
#pragma unroll
        for (int cb = 0; cb < 4; cb++) *(LDS f32x4*)(size_t)(pb + (a * 4 + cb) * 1024 + lane * 16) = acc[a][cb];
    }
    waitl(); bar();
  } else {
    __builtin_amdgcn_s_setprio(1);
    if (w == 0) {
      const int lane = fresh_lane();
      int rt = (rb0 >> 2) + (lane >> 3), ct = lane & 7;
      bool pub = lane < 32 && rt <= ((rb0 + nblk - 1) >> 2);
      int cons = pub ? ((rt >> 2) + 8 * ((rt & 3) + 4 * ct)) : bid;
      u64 tg = __hip_atomic_load(epoch + cons * 8, __ATOMIC_RELAXED, __HIP_MEMORY_SCOPE_AGENT) + 1;
      *(LDS u64*)(size_t)(L_TAG + lane * 8) = tg;
      waitl();
    }
    if (is_mlp) {
      if (nblk == 11) mlp_path<11>(p, w, rb0, S TSPASS);
      else mlp_path<10>(p, w, rb0, S TSPASS);
    } else qkv_path(p, w, P, S TSPASS);
    STAMP(3);
    const int lane = fresh_lane(), i = lane & 15, q = lane >> 4;
    W2_OFFS();
    const u64 tg = *(const LDS u64*)(size_t)(L_TAG + lane * 8);
    const u64 mytag = *(const LDS u64*)(size_t)(L_TAG + 63 * 8);
    waitv<0>();
    unsigned old = 0;
    if (lane == 0) old = __hip_atomic_fetch_add((LDS unsigned*)(size_t)L_SYNC, 1u, __ATOMIC_RELAXED, __HIP_MEMORY_SCOPE_WORKGROUP);
    if (__builtin_amdgcn_readfirstlane(old) == 3) {
      int rt = (rb0 >> 2) + (lane >> 3), ct = lane & 7;
      if (lane < 32 && rt <= ((rb0 + nblk - 1) >> 2)) {
        int cons = (rt >> 2) + 8 * ((rt & 3) + 4 * ct);
        int slot = is_mlp ? ((P & 1) * 14 + S) : (28 + S);
        __hip_atomic_store(flags + cons * 64 + slot, tg, __ATOMIC_RELAXED, __HIP_MEMORY_SCOPE_AGENT);
      }
    }
    STAMP(4);
    const int otok = tok_of(4 * rt2 + w, i);
    u32x2 xres[4];
#pragma unroll
    for (int cb = 0; cb < 4; cb++) xres[cb] = *(const u32x2*)(p.x + otok * 1024 + ct2 * 128 + cb * 32 + q * 8);
    f32x4 as[4], ad[4];
#pragma unroll
    for (int cb = 0; cb < 4; cb++) { as[cb] = f32x4{0.f, 0.f, 0.f, 0.f}; ad[cb] = f32x4{0.f, 0.f, 0.f, 0.f}; }
    const unsigned abase = (rt2 * 72 * 4 + w) * 1024;
    poll_flags(flags + bid * 64 + lane, lane >= 28 && lane < 36, mytag);
    {
      i32x4 af[16];
#pragma unroll
      for (int n = 0; n < 16; n++) af[n] = __builtin_amdgcn_raw_buffer_load_b128(ra, lane * 16, abase + n * 4096, 16);
      lds_spin4(L_SYNC + 16);
#pragma unroll
      for (int n = 0; n < 16; n++) {
        const unsigned pg = zpage(n >> 2, n & 3) * 4096;
#pragma unroll
        for (int cb = 0; cb < 4; cb++) as[cb] = mfma32(lds_tr(pg + wb[cb], pg + wb[cb] + 2048), __builtin_bit_cast(s16x8, af[n]), as[cb]);
      }
    }
    waitl();
    if (lane == 0) *(volatile LDS unsigned*)(size_t)(L_SYNC + 48 + w * 4) = 1;
    STAMP(5);
    {
      const int b0 = 4 * rt2 + w;
      const int pl = 3 * (b0 >> 5) + (b0 & 31) / 11;
      const int lo = (pl & 1) * 14 + 9;
      poll_flags(flags + bid * 64 + lane, lane >= lo && lane < lo + 5, mytag);
    }
    STAMP(6);
    {
      i32x4 af[20];
#pragma unroll
      for (int n = 0; n < 20; n++) af[n] = __builtin_amdgcn_raw_buffer_load_b128(ra, lane * 16, abase + (52 + n) * 4096, 16);
      lds_spin4(L_SYNC + 32);
#pragma unroll
      for (int n = 0; n < 20; n++) {
        const unsigned pg = zpage(4 + (n >> 2), n & 3) * 4096;
#pragma unroll
        for (int cb = 0; cb < 4; cb++) ad[cb] = mfma32(lds_tr(pg + wb[cb], pg + wb[cb] + 2048), __builtin_bit_cast(s16x8, af[n]), ad[cb]);
      }
    }
    STAMP(7);
    bar();
    if (w == 0 && lane == 0) __hip_atomic_store(epoch + bid * 8, mytag, __ATOMIC_RELAXED, __HIP_MEMORY_SCOPE_AGENT);
#pragma unroll
    for (int cb = 0; cb < 4; cb++)
#pragma unroll
      for (int l = 0; l < 4; l++) ad[cb] += ldsf4(zpage(l, 0) * 4096 + (w * 4 + cb) * 1024 + lane * 16);
    unsigned o[4][2];
#pragma unroll
    for (int cb = 0; cb < 4; cb++)
#pragma unroll
      for (int h = 0; h < 2; h++) {
        f32x2 d = h ? hi2(ad[cb]) : lo2(ad[cb]), s = h ? hi2(as[cb]) : lo2(as[cb]);
        f32x2 e = d * sp2(-LOG2E);
        e[0] = __builtin_amdgcn_exp2f(e[0]); e[1] = __builtin_amdgcn_exp2f(e[1]);
        e = e + sp2(1.f);
        e[0] = __builtin_amdgcn_rcpf(e[0]); e[1] = __builtin_amdgcn_rcpf(e[1]);
        f32x2 xv = {__uint_as_float(xres[cb][h] << 16), __uint_as_float(xres[cb][h] & 0xffff0000u)};
        o[cb][h] = pack2(fma2(s * d, e, xv));
      }
#pragma unroll
    for (int c2 = 0; c2 < 2; c2++) {
      auto a = __builtin_amdgcn_permlane16_swap(o[2 * c2][0], o[2 * c2 + 1][0], false, false);
      auto b = __builtin_amdgcn_permlane16_swap(o[2 * c2][1], o[2 * c2 + 1][1], false, false);
      u32x4 v = {a[0], b[0], a[1], b[1]};
      asm volatile("global_store_dwordx4 %0, %1, %2 sc1\n\ts_nop 1" :: "v"(otok * 1024 + ct2 * 128 + c2 * 64 + (q & 1) * 32 + (q >> 1) * 16), "v"(v), "s"(p.out) : "memory");
    }
  }
  STAMP(8);
}

extern "C" void kernel_launch(void* const* d_in, const int* in_sizes, int n_in, void* d_out, int out_size,
                              void* d_ws, size_t ws_size, hipStream_t stream) {
  Params p;
  p.x = (const char*)d_in[0]; p.wqkv = (const char*)d_in[1]; p.wo = (const char*)d_in[2];
  p.wg = (const char*)d_in[4]; p.wu = (const char*)d_in[5]; p.wd = (const char*)d_in[6];
  p.out = (char*)d_out; p.ws = (char*)d_ws;
  fused_kernel<<<256, 512, 0, stream>>>(p);
}
```

```cpp
#include <hip/hip_runtime.h>
#include <hip/hip_bf16.h>

typedef short s16x4 __attribute__((ext_vector_type(4)));
typedef short s16x8 __attribute__((ext_vector_type(8)));
typedef __bf16 bf2 __attribute__((ext_vector_type(2)));
typedef float f32x2 __attribute__((ext_vector_type(2)));
typedef float f32x4 __attribute__((ext_vector_type(4)));
typedef int i32x4 __attribute__((ext_vector_type(4)));
typedef unsigned u32x2 __attribute__((ext_vector_type(2)));
typedef unsigned u32x4 __attribute__((ext_vector_type(4)));
typedef unsigned long long u64;
#define LDS __attribute__((address_space(3)))
#define GLB __attribute__((address_space(1)))
#define DEV __device__ __forceinline__
#define SB() __builtin_amdgcn_sched_barrier(0)
template <int V> struct IC { static constexpr int v = V; };
template <int I, int N, class F> DEV void sfor(F&& f) { if constexpr (I < N) { f(IC<I>{}); sfor<I + 1, N>(f); } }

constexpr int L_X = 65536;
constexpr int XSLOT = 24576;
constexpr int L_HALO = 139264;
constexpr int L_MISC = 155648;
constexpr int L_STAT = L_MISC;
constexpr int L_TAG = L_MISC + 3584;
constexpr int L_PAY = L_MISC + 3648;
constexpr int L_SYNC = L_MISC + 4096;
constexpr int L_JUNK = L_MISC + 5120;
constexpr int LDS_BYTES = 163840;

constexpr size_t WS_A = 0;
constexpr unsigned FRG = 2048, RBS = 72 * FRG;
constexpr size_t WS_A_BYTES = 128u * RBS;
constexpr size_t WS_FLAG = WS_A_BYTES;
constexpr unsigned FSTR = 36 * 128;
constexpr size_t WS_DONE = WS_FLAG + 32 * FSTR;
constexpr size_t WS_GRAN = WS_DONE + 2048;
constexpr size_t WS_EPOCH = WS_GRAN + 32768;

constexpr unsigned SPIN_CAP = 1u << 18;

struct Params {
  const char* x; const char* wqkv; const char* wo; const char* wg; const char* wu; const char* wd;
  char* out; char* ws;
};

DEV i32x4 make_srd(const void* p, unsigned bytes) {
  u64 a = (u64)p;
  i32x4 r; r[0] = (int)(unsigned)a; r[1] = (int)((unsigned)(a >> 32) & 0xffffu); r[2] = (int)bytes; r[3] = 0x00020000;
  return r;
}
DEV void dma16(unsigned lds, unsigned voff, i32x4 srd, unsigned soff) {
  asm volatile("s_mov_b32 m0, %0\n\ts_nop 0\n\tbuffer_load_dwordx4 %1, %2, %3 offen lds"
               :: "s"(lds), "v"(voff), "s"(srd), "s"(soff) : "memory");
}
DEV void dma16m(unsigned lds, unsigned voff, i32x4 srd, unsigned soff, u64 mask) {
  asm volatile("s_mov_b32 m0, %0\n\ts_mov_b64 exec, %4\n\tbuffer_load_dwordx4 %1, %2, %3 offen lds\n\ts_mov_b64 exec, -1"
               :: "s"(lds), "v"(voff), "s"(srd), "s"(soff), "s"(mask) : "memory");
}
DEV void dma_x3(unsigned lds, unsigned v0, unsigned v1, unsigned v2, i32x4 srd, unsigned soff) {
  asm volatile("s_mov_b32 m0, %0\n\ts_nop 0\n\tbuffer_load_dwordx4 %1, %4, %5 offen lds\n\t"
               "buffer_load_dwordx4 %2, %4, %5 offen offset:1024 lds\n\tbuffer_load_dwordx4 %3, %4, %5 offen offset:2048 lds"
               :: "s"(lds), "v"(v0), "v"(v1), "v"(v2), "s"(srd), "s"(soff) : "memory");
}
DEV void dma_x3m(unsigned lds, unsigned v0, unsigned v1, unsigned v2, i32x4 srd, unsigned soff, u64 mask) {
  asm volatile("s_mov_b32 m0, %0\n\ts_mov_b64 exec, %6\n\tbuffer_load_dwordx4 %3, %4, %5 offen offset:2048 lds\n\ts_mov_b64 exec, -1\n\t"
               "buffer_load_dwordx4 %1, %4, %5 offen lds\n\tbuffer_load_dwordx4 %2, %4, %5 offen offset:1024 lds"
               :: "s"(lds), "v"(v0), "v"(v1), "v"(v2), "s"(srd), "s"(soff), "s"(mask) : "memory");
}
DEV void dma_w4(unsigned lds, unsigned v0, unsigned v1, i32x4 srd0, i32x4 srd1, unsigned soff, u64 mask) {
  asm volatile("s_mov_b32 m0, %0\n\ts_mov_b64 exec, %6\n\tbuffer_load_dwordx4 %1, %3, %5 offen lds\n\t"
               "buffer_load_dwordx4 %2, %3, %5 offen offset:1024 lds\n\ts_bitset1_b32 m0, 13\n\ts_nop 0\n\t"
               "buffer_load_dwordx4 %1, %4, %5 offen lds\n\tbuffer_load_dwordx4 %2, %4, %5 offen offset:1024 lds\n\ts_mov_b64 exec, -1"
               :: "s"(lds), "v"(v0), "v"(v1), "s"(srd0), "s"(srd1), "s"(soff), "s"(mask) : "memory");
}
DEV void dma_p4(unsigned lds, const unsigned (&v)[4], i32x4 srd, unsigned soff) {
  asm volatile("s_mov_b32 m0, %0\n\ts_nop 0\n\tbuffer_load_dwordx4 %1, %5, %6 offen lds\n\t"
               "buffer_load_dwordx4 %2, %5, %6 offen offset:1024 lds\n\tbuffer_load_dwordx4 %3, %5, %6 offen offset:2048 lds\n\t"
               "buffer_load_dwordx4 %4, %5, %6 offen offset:3072 lds"
               :: "s"(lds), "v"(v[0]), "v"(v[1]), "v"(v[2]), "v"(v[3]), "s"(srd), "s"(soff) : "memory");
}
DEV void dma_p2(unsigned lds, unsigned v0, unsigned v1, i32x4 srd, unsigned soff) {
  asm volatile("s_mov_b32 m0, %0\n\ts_nop 0\n\tbuffer_load_dwordx4 %1, %3, %4 offen lds\n\tbuffer_load_dwordx4 %2, %3, %4 offen offset:1024 lds"
               :: "s"(lds), "v"(v0), "v"(v1), "s"(srd), "s"(soff) : "memory");
}
DEV void store16_sc1(i32x4 v, unsigned voff, i32x4 srd, unsigned soff) {
  asm volatile("buffer_store_dwordx4 %0, %1, %2, %3 offen sc1\n\ts_nop 1" :: "v"(v), "v"(voff), "s"(srd), "s"(soff) : "memory");
}
template <int N> DEV void waitv() { asm volatile("s_waitcnt vmcnt(%0)" :: "n"(N) : "memory"); }
DEV void waitl() { __builtin_amdgcn_s_waitcnt(0xC07F); asm volatile("" ::: "memory"); }
DEV void bar() { asm volatile("s_barrier" ::: "memory"); }
DEV s16x8 lds_b128(unsigned a) { return *(const LDS s16x8*)(size_t)a; }
DEV s16x4 lds_tr4(unsigned a) { return __builtin_amdgcn_ds_read_tr16_b64_v4i16((LDS s16x4*)(size_t)a); }
DEV s16x8 cat(s16x4 lo, s16x4 hi) { return __builtin_shufflevector(lo, hi, 0, 1, 2, 3, 4, 5, 6, 7); }
DEV f32x4 ldsf4(unsigned a) { return *(const LDS f32x4*)(size_t)a; }
DEV float ldsf(unsigned a) { return *(const LDS float*)(size_t)a; }
DEV f32x4 mfma16(u32x2 a, u32x2 b, f32x4 c) {
  return __builtin_amdgcn_mfma_f32_16x16x16bf16_1k(__builtin_bit_cast(s16x4, a), __builtin_bit_cast(s16x4, b), c, 0, 0, 0);
}
DEV unsigned pack2(float a, float b) { bf2 p = {(__bf16)a, (__bf16)b}; return __builtin_bit_cast(unsigned, p); }
DEV unsigned pack2(f32x2 a) { return pack2(a[0], a[1]); }
DEV float rbf(float a) { return (float)(__bf16)a; }
DEV f32x2 lo2(f32x4 a) { return f32x2{a[0], a[1]}; }
DEV f32x2 hi2(f32x4 a) { return f32x2{a[2], a[3]}; }
DEV f32x2 fma2(f32x2 a, f32x2 b, f32x2 c) { return __builtin_elementwise_fma(a, b, c); }
DEV f32x2 sp2(float a) { return f32x2{a, a}; }
DEV int fresh_lane() { int l; asm volatile("v_mbcnt_lo_u32_b32 %0, -1, 0\n\tv_mbcnt_hi_u32_b32 %0, -1, %0" : "=v"(l)); return l; }
DEV int ssel(bool c, int a, int b) { asm volatile("" : "+s"(a), "+s"(b)); return c ? a : b; }
DEV unsigned vsel(bool c, unsigned a, unsigned b) { asm volatile("" : "+v"(a), "+v"(b)); return c ? a : b; }
DEV unsigned frag_off(int la) { return (la >> 3) * 256 + (la & 7) * 16; }
DEV int tok_of(int rb, int i) { return 32 * (rb >> 1) + 2 * i + (rb & 1); }
DEV void lds_spin4(unsigned a) {
  for (;;) {
    u32x4 v = *(volatile LDS u32x4*)(size_t)a;
    if (__builtin_amdgcn_readfirstlane(v[0] & v[1] & v[2] & v[3])) break;
    __builtin_amdgcn_s_sleep(1);
  }
  asm volatile("" ::: "memory");
}
constexpr float LOG2E = 1.4426950408889634f;

template <int CTRL> DEV float dpp_add(float v) {
  return v + __int_as_float(__builtin_amdgcn_mov_dpp(__float_as_int(v), CTRL, 0xf, 0xf, true));
}
DEV float sum32(float v) {
  v = dpp_add<0x128>(v); v = dpp_add<0x124>(v); v = dpp_add<0x122>(v); v = dpp_add<0x121>(v);
  auto b = __builtin_amdgcn_permlane16_swap(__float_as_uint(v), __float_as_uint(v), false, false);
  return __uint_as_float(b[0]) + __uint_as_float(b[1]);
}
DEV void stat_publish(int w, int lane) {
  struct { u32x4 a, b; } t;
  waitv<0>();
  t.a = *(const LDS u32x4*)(size_t)(49152 + w * 4096 + (lane >> 5) * 1024 + (lane & 31) * 16);
  t.b = *(const LDS u32x4*)(size_t)(49152 + w * 4096 + (lane >> 5) * 1024 + (lane & 31) * 16 + 512);
  f32x2 sv[8];
#pragma unroll
  for (int j = 0; j < 8; j++) {
    const unsigned u = j < 4 ? t.a[j & 3] : t.b[j & 3];
    sv[j] = f32x2{__uint_as_float(u << 16), __uint_as_float(u & 0xffff0000u)};
  }
  const f32x2 a2 = ((sv[0] + sv[1]) + (sv[2] + sv[3])) + ((sv[4] + sv[5]) + (sv[6] + sv[7]));
  const float mean = sum32(a2[0] + a2[1]) * (1.f / 512.f);
#pragma unroll
  for (int j = 0; j < 8; j++) { sv[j] = sv[j] - sp2(mean); sv[j] = sv[j] * sv[j]; }
  const f32x2 v2 = ((sv[0] + sv[1]) + (sv[2] + sv[3])) + ((sv[4] + sv[5]) + (sv[6] + sv[7]));
  const float var = sum32(v2[0] + v2[1]) * (1.f / 512.f);
  *(LDS unsigned*)(size_t)(L_PAY + (2 * w + (lane >> 5)) * 4) = pack2(-mean, __builtin_amdgcn_sqrtf(rbf(rbf(var) + 1e-5f)));
  waitl();
  __builtin_amdgcn_s_setprio(1);
}

template <int NB, int NC>
struct S1 { f32x4 acc[NB][NC]; };

DEV void mfma_acc(f32x4& c, s16x8 a, s16x8 b) {
  asm volatile("v_mfma_f32_16x16x32_bf16 %0, %1, %2, %0" : "+v"(c) : "v"(a), "v"(b));
}
DEV void mfma_new(f32x4& c, s16x8 a, s16x8 b) { asm volatile("v_mfma_f32_16x16x32_bf16 %0, %1, %2, 0" : "=&v"(c) : "v"(a), "v"(b)); }
template <bool FIRST> DEV void mfma_st(f32x4& c, s16x8 a, s16x8 b) { if (FIRST) mfma_new(c, a, b); else mfma_acc(c, a, b); }
template <bool QKV> DEV s16x4 w_half(int idx, unsigned wa0, unsigned wa1) {
  const int c = idx >> 1, t = idx & 1;
  if (QKV) return lds_tr4(wa0 + c * 128 + t * 8192);
  return lds_tr4(((c & 1) ? wa1 : wa0) + (c >> 1) * 8192 + t * 1024);
}
template <bool QKV> DEV s16x8 x_frag(int b, unsigned xa, unsigned ha) {
  if (QKV && b == 0) return lds_b128(ha);
  const int k = QKV ? b - 1 : b;
  return lds_b128(xa + (k & 1) * 12288 + (k >> 1) * 1024);
}
constexpr int PF = 4;
template <bool QKV, int NB, int NC, class F, class G>
DEV void c_main(S1<NB, NC>& st, unsigned xa, unsigned ha, unsigned wa0, unsigned wa1, F&& pre, G&& late) {
  unsigned xb[2][2] = {{L_X + xa, L_X + 2 * XSLOT + xa}, {L_X + (xa ^ 64), L_X + 2 * XSLOT + (xa ^ 64)}}, hb[2] = {L_HALO + ha, L_HALO + (ha ^ 64)};
  asm volatile("" : "+v"(xb[0][0]), "+v"(xb[0][1]), "+v"(xb[1][0]), "+v"(xb[1][1]), "+v"(wa0));
  if (QKV) asm volatile("" : "+v"(hb[0]), "+v"(hb[1])); else asm volatile("" : "+v"(wa1));
  auto xrd = [&](int s, int b) {
    const int sl = (s >> 1) % 3;
    return x_frag<QKV>(b, xb[s & 1][sl >> 1] + (sl & 1) * XSLOT, hb[s & 1] + (s >> 1) * 2048);
  };
  auto wrd = [&](int s, int h) { return w_half<QKV>(h, wa0 + (s % 3) * 16384, wa1 + (s % 3) * 16384); };
  SB();
  pre();
  bar();
  s16x8 xf[16 * NB]; s16x4 wh[16][2 * NC];
#pragma unroll
  for (int h = 0; h < 2 * NC; h++) wh[0][h] = wrd(0, h);
#pragma unroll
  for (int j = 0; j < PF; j++) xf[j] = xrd(0, j);
  waitl();
  sfor<0, 16>([&](auto sc) __attribute__((always_inline)) {
    constexpr int s = decltype(sc)::v;
    if (s == 1) { late(); SB(); }
    bar();
#pragma unroll
    for (int b = 0; b < NB; b++)
#pragma unroll
      for (int c = 0; c < NC; c++) {
        int e = ((b >> 1) & 1) ? NC - 1 - c : c, bb = b;
        if ((b | 1) < NB) {
          const int t = (b & 1) * NC + c, cc = t >> 1;
          e = ((b >> 1) & 1) ? NC - 1 - cc : cc; bb = (b & ~1) + ((t ^ cc) & 1);
        }
        const s16x8 wf = cat(wh[s][2 * e], wh[s][2 * e + 1]);
        if (QKV && e == 2) mfma_st<s == 0>(st.acc[bb][e], xf[s * NB + bb], wf); else mfma_st<s == 0>(st.acc[bb][e], wf, xf[s * NB + bb]);
        if (s < 15 && c < 2 && b < NC) wh[s < 15 ? s + 1 : 0][2 * b + c] = wrd(s + 1, 2 * b + c);
        const int n = s * NB + b + PF;
        if (c == 2 && n < 16 * NB) xf[n < 16 * NB ? n : 0] = xrd(n / NB, n % NB);
        SB();
      }
  });
  asm volatile("s_nop 7\n\ts_barrier" ::: "memory");
  SB();
}
template <int L> DEV float row_bcast(float v) {
  return __int_as_float(__builtin_amdgcn_mov_dpp(__float_as_int(v), 0x150 + L, 0xf, 0xf, true));
}
template <int L> DEV f32x4 row_bcast4(f32x4 v) { return f32x4{row_bcast<L>(v[0]), row_bcast<L>(v[1]), row_bcast<L>(v[2]), row_bcast<L>(v[3])}; }
DEV unsigned lds_arrive(unsigned a, int lane) {
  unsigned old = 0;
  if (lane == 0) old = __hip_atomic_fetch_add((LDS unsigned*)(size_t)a, 1u, __ATOMIC_RELAXED, __HIP_MEMORY_SCOPE_WORKGROUP);
  return old;
}
DEV void flag_store(unsigned voff, u32x4 tag, i32x4 srd) {
  asm volatile("buffer_store_dwordx4 %0, %1, %2, 0 offen sc1\n\ts_nop 1" :: "v"(tag), "v"(voff), "s"(srd) : "memory");
}
DEV void mlp_path(const Params& p, int w, int P, int P3, int t3, int S) {
  constexpr int NB = 11;
  const int lane = fresh_lane(), i = lane & 15, q = lane >> 4;
  auto pre = [&]() __attribute__((always_inline)) { stat_publish(w, lane); };
  if (w == 1 && lane < 24) *(LDS u32x4*)(size_t)(L_X + (lane >> 3) * XSLOT + 11 * 1024 + 7 * 128 + (lane & 7) * 16) = u32x4{0x3f803f80u, 0x3f803f80u, 0x3f803f80u, 0x3f803f80u};
  if (w == 0 && lane < 32) *(LDS unsigned*)(size_t)(L_SYNC + lane * 4) = 0;
  S1<NB, 4> st;
  const unsigned ha = i * 128 + ((q ^ (i >> 1)) & 7) * 16, xa = ha + (i >> 3) * 5120;
  const int krow = 8 * q + (i >> 2);
  const int f = (krow & 3) | (((krow >> 3) & 1) << 2);
  const unsigned wa0 = krow * 256 + (((2 * w) ^ f) & 7) * 32 + (i & 3) * 8;
  unsigned ta, vst, vmix, vfl, ksoff, soff; i32x4 sa;
  auto late = [&]() __attribute__((always_inline)) {
    const int lane = fresh_lane(), i = lane & 15, q = lane >> 4, B = 32 * P3;
    ta = L_STAT + i * 16; vst = frag_off(lane);
    const int rA = ssel(t3 == 0, 10, 21), dA = ssel(t3 == 2, 8, 0), dB = ssel(t3 == 1, 4, 0), lim = ssel(t3 == 2, 8, 12);
    vmix = vsel(i < lim, (B + (i < 8 ? rA : 10)) * RBS + frag_off(q * 16 + i + (i < 8 ? dA : dB)), 0x7ff00000u);
    sa = make_srd(p.ws + WS_A, (unsigned)(WS_A_BYTES + 32 * FSTR));
    ksoff = (16 + 4 * S + w) * FRG; soff = (B + 11 * t3) * RBS + ksoff;
    vfl = vsel(lane < ssel(t3 == 1, 32, 24), (unsigned)WS_FLAG + (8 * P3 + ((5 * t3) >> 1) + (lane >> 3)) * FSTR + ((P & 1) * 14 + S) * 128 + (lane & 7) * 16, 0x7ff00000u);
    asm volatile("" : "+v"(ta), "+v"(vst), "+v"(vmix), "+v"(vfl));
    asm volatile("" : "+s"(sa), "+s"(soff), "+s"(ksoff));
  };
  c_main<false, NB, 4>(st, xa, 0u, wa0, wa0 ^ 32, pre, late);
  f32x4 cs[4];
#pragma unroll
  for (int c = 0; c < 4; c++) cs[c] = row_bcast4<15>(st.acc[10][c]);
  f32x4 stv[NB + 2];
  stv[0] = ldsf4(ta); stv[1] = ldsf4(ta + 256);
  const u32x4 tag = *(const LDS u32x4*)(size_t)L_TAG;
#pragma unroll
  for (int b = 0; b < NB; b++) {
    if (b + 2 < NB) stv[b + 2] = ldsf4(ta + (b + 2) * 256);
    SB();
    asm volatile(
      "v_pk_fma_f32 v[240:241], %8, %16, %0 op_sel_hi:[1,0,1]\n\t"
      "v_pk_fma_f32 v[242:243], %9, %16, %1 op_sel_hi:[1,0,1]\n\t"
      "v_pk_fma_f32 v[244:245], %10, %16, %2 op_sel_hi:[1,0,1]\n\t"
      "v_pk_fma_f32 v[246:247], %11, %16, %3 op_sel_hi:[1,0,1]\n\t"
      "v_pk_mul_f32 v[240:241], v[240:241], %17 op_sel_hi:[1,0]\n\t"
      "v_pk_mul_f32 v[242:243], v[242:243], %17 op_sel_hi:[1,0]\n\t"
      "v_pk_mul_f32 v[244:245], v[244:245], %17 op_sel_hi:[1,0]\n\t"
      "v_pk_mul_f32 v[246:247], v[246:247], %17 op_sel_hi:[1,0]\n\t"
      "v_exp_f32 v240, v240\n\tv_exp_f32 v241, v241\n\t"
      "v_pk_fma_f32 v[248:249], %12, %16, %4 op_sel_hi:[1,0,1]\n\t"
      "v_exp_f32 v242, v242\n\tv_exp_f32 v243, v243\n\t"
      "v_pk_fma_f32 v[250:251], %13, %16, %5 op_sel_hi:[1,0,1]\n\t"
      "v_exp_f32 v244, v244\n\tv_exp_f32 v245, v245\n\t"
      "v_pk_fma_f32 v[240:241], v[240:241], %16, %16 op_sel:[0,1,1]\n\t"
      "v_exp_f32 v246, v246\n\tv_exp_f32 v247, v247\n\t"
      "v_pk_fma_f32 v[242:243], v[242:243], %16, %16 op_sel:[0,1,1]\n\t"
      "v_rcp_f32 v240, v240\n\tv_rcp_f32 v241, v241\n\t"
      "v_pk_fma_f32 v[244:245], v[244:245], %16, %16 op_sel:[0,1,1]\n\t"
      "v_pk_fma_f32 v[246:247], v[246:247], %16, %16 op_sel:[0,1,1]\n\t"
      "v_rcp_f32 v242, v242\n\tv_rcp_f32 v243, v243\n\t"
      "v_pk_fma_f32 v[252:253], %14, %16, %6 op_sel_hi:[1,0,1]\n\t"
      "v_pk_mul_f32 v[248:249], v[248:249], v[240:241]\n\t"
      "v_rcp_f32 v244, v244\n\tv_rcp_f32 v245, v245\n\t"
      "v_pk_fma_f32 v[254:255], %15, %16, %7 op_sel_hi:[1,0,1]\n\t"
      "v_pk_mul_f32 v[250:251], v[250:251], v[242:243]\n\t"
      "v_rcp_f32 v246, v246\n\tv_rcp_f32 v247, v247\n\t"
      "v_cvt_pk_bf16_f32 v248, v248, v249\n\t"
      "v_pk_mul_f32 v[252:253], v[252:253], v[244:245]\n\t"
      "v_pk_mul_f32 v[254:255], v[254:255], v[246:247]\n\t"
      "v_cvt_pk_bf16_f32 v249, v250, v251\n\t"
      "v_cvt_pk_bf16_f32 v250, v252, v253\n\t"
      "v_cvt_pk_bf16_f32 v251, v254, v255\n\t"
      "buffer_store_dwordx4 v[248:251], %18, %19, %20 offen sc1"
      :: "v"(lo2(st.acc[b][0])), "v"(hi2(st.acc[b][0])), "v"(lo2(st.acc[b][1])), "v"(hi2(st.acc[b][1])),
         "v"(lo2(st.acc[b][2])), "v"(hi2(st.acc[b][2])), "v"(lo2(st.acc[b][3])), "v"(hi2(st.acc[b][3])),
         "v"(lo2(cs[0])), "v"(hi2(cs[0])), "v"(lo2(cs[1])), "v"(hi2(cs[1])),
         "v"(lo2(cs[2])), "v"(hi2(cs[2])), "v"(lo2(cs[3])), "v"(hi2(cs[3])),
         "v"(lo2(stv[b])), "v"(hi2(stv[b])), "v"(b < 10 ? vst : vmix), "s"(sa), "s"(b < 10 ? soff + b * RBS : ksoff)
      : "memory", "v240", "v241", "v242", "v243", "v244", "v245", "v246", "v247", "v248", "v249", "v250", "v251", "v252", "v253", "v254", "v255");
    SB();
  }
  waitv<0>();
  if (__builtin_amdgcn_readfirstlane(lds_arrive(L_SYNC, vst)) == 3) flag_store(vfl, tag, sa);
}

DEV void qkv_path(const Params& p, int w, int P, int G) {
  const int lane = fresh_lane(), i = lane & 15, q = lane >> 4;
  auto pre = [&]() __attribute__((always_inline)) { stat_publish(w, lane); };
  if (w == 1) *(LDS u32x4*)(size_t)(L_HALO + (lane >> 3) * 2048 + (lane & 7) * 16) = u32x4{0x3f803f80u, 0x3f803f80u, 0x3f803f80u, 0x3f803f80u};
  if (w == 0 && lane < 32) *(LDS unsigned*)(size_t)(L_SYNC + lane * 4) = 0;
  S1<13, 3> st;
  const unsigned ha = i * 128 + ((q ^ (i >> 1)) & 7) * 16, xa = ha + (i >> 3) * 5120;
  const int krow = 8 * q + (i >> 2);
  const int f = ((krow >> 1) & 1) | (((krow >> 3) & 1) << 1);
  const unsigned wa0 = q * 2048 + (i >> 3) * 1024 + ((i >> 2) & 1) * 384 + ((w ^ f) & 3) * 32 + (i & 3) * 8;
  f32x4 mcur, mprev, mh0, mh1; u32x2 selc; unsigned voff, ti, tq, vfl, sbase; int dq; i32x4 sa;
  auto late = [&]() __attribute__((always_inline)) {
  const int lane = fresh_lane(), i = lane & 15, q = lane >> 4, head = 4 * G + w;
  const float NEG = -__builtin_inff();
  const bool first = (P == 0);
#pragma unroll
  for (int r = 0; r < 4; r++) {
    int kap = 4 * q + r;
    mcur[r] = (kap >= i - 7) ? 0.f : NEG;
    mprev[r] = (kap >= i + 9) ? 0.f : NEG;
    mh0[r] = (!first && kap < 8) ? 0.f : NEG;
    mh1[r] = first ? NEG : mprev[r];
  }
  selc[0] = ((4 * q <= i) ? 0xffffu : 0u) | ((4 * q + 1 <= i) ? 0xffff0000u : 0u);
  selc[1] = ((4 * q + 2 <= i) ? 0xffffu : 0u) | ((4 * q + 3 <= i) ? 0xffff0000u : 0u);
  voff = (q & 1) * RBS + frag_off((2 * (head & 1) + (q >> 1)) * 16 + i);
  ti = L_STAT + i * 4; tq = L_STAT + q * 16;
  dq = i - 4 * q;
  sa = make_srd(p.ws + WS_A, (unsigned)(WS_A_BYTES + 32 * FSTR));
  vfl = lane < (P < 10 ? 24 : 16) ? (unsigned)WS_FLAG + (3 * P + (lane >> 3)) * FSTR + (28 + G) * 128 + (lane & 7) * 16 : 0x7ff00000u;
  asm volatile("" : "+v"(vfl));
  sbase = 12 * P * RBS + (head >> 1) * FRG;
  asm volatile("" : "+s"(sa), "+s"(sbase));
  asm volatile("" : "+v"(mcur), "+v"(mprev), "+v"(mh0), "+v"(mh1), "+v"(selc), "+v"(voff), "+v"(ti), "+v"(tq), "+v"(dq));
  };
  c_main<true, 13, 3>(st, xa, ha, wa0, wa0, pre, late);
  float nm[13], rs[13]; f32x4 m4[13], r4[13];
  auto rd = [&](int b) {
    const int tb = (b == 0 ? 12 : b - 1) * 128;
    nm[b] = ldsf(ti + tb); rs[b] = ldsf(ti + tb + 64); m4[b] = ldsf4(tq + tb); r4[b] = ldsf4(tq + tb + 64);
  };
  const float csv = __int_as_float(__builtin_amdgcn_ds_bpermute(ti - L_STAT, __float_as_int(st.acc[0][2][0])));
  rd(0); rd(1); rd(2);
  const u32x4 tag = *(const LDS u32x4*)(size_t)L_TAG;
  const f32x4 csq = row_bcast4<0>(st.acc[0][0]), csk = row_bcast4<0>(st.acc[0][1]);
  const bool c0 = dq >= 0, c1 = dq >= 1, c2 = dq >= 2, c3 = dq >= 3;
  u32x2 Q[13], K[13], V[13]; float qs[13];
  auto fold = [&](int b) {
    if (b + 3 < 13) rd(b + 3);
    SB();
    f32x2 t[6];
    t[0] = fma2(lo2(csk), sp2(nm[b]), lo2(st.acc[b][1])); t[1] = fma2(hi2(csk), sp2(nm[b]), hi2(st.acc[b][1]));
    t[2] = fma2(lo2(m4[b]), sp2(csv), lo2(st.acc[b][2])); t[3] = fma2(hi2(m4[b]), sp2(csv), hi2(st.acc[b][2]));
    if (b > 0) { t[4] = fma2(lo2(csq), sp2(nm[b]), lo2(st.acc[b][0])); t[5] = fma2(hi2(csq), sp2(nm[b]), hi2(st.acc[b][0])); }
    t[0] *= sp2(rs[b]); t[1] *= sp2(rs[b]); t[2] *= lo2(r4[b]); t[3] *= hi2(r4[b]);
    if (b > 0) { Q[b] = u32x2{pack2(t[4]), pack2(t[5])}; qs[b] = rs[b] * (0.25f * LOG2E); }
    K[b] = u32x2{pack2(t[0]), pack2(t[1])};
    V[b] = u32x2{pack2(t[2]), pack2(t[3])};
    SB();
  };
  fold(0);
  const u32x2 ones = {0x3f803f80u, 0x3f803f80u};
  const f32x4 zero = {0.f, 0.f, 0.f, 0.f};
#pragma unroll
  for (int g2 = 0; g2 < 3; g2++) {
    f32x4 sc[4], sp[4]; float mx[4]; u32x2 pk[4]; f32x4 o[4], sm[4];
#pragma unroll
    for (int u = 0; u < 4; u++) {
      const int b = 1 + 4 * g2 + u, pb = (b <= 2) ? 0 : b - 2;
      fold(b);
      sc[u] = mfma16(K[b], Q[b], mcur);
      sp[u] = mfma16(K[pb], Q[b], b == 1 ? mh0 : (b == 2 ? mh1 : mprev));
    }
#pragma unroll
    for (int u = 0; u < 4; u++) {
      sc[u] = f32x4{c0 ? sc[u][0] : sp[u][0], c1 ? sc[u][1] : sp[u][1], c2 ? sc[u][2] : sp[u][2], c3 ? sc[u][3] : sp[u][3]};
    }
    asm volatile("v_max3_f32 %0, %4, %5, %6\n\tv_max3_f32 %1, %8, %9, %10\n\tv_max3_f32 %2, %12, %13, %14\n\tv_max3_f32 %3, %16, %17, %18\n\t"
                 "v_max_f32 %0, %0, %7\n\tv_max_f32 %2, %2, %15\n\tv_max_f32 %1, %1, %11\n\tv_max_f32 %3, %3, %19\n\t"
                 "s_nop 0\n\t"
                 "v_permlane32_swap_b32 %0, %2\n\tv_permlane32_swap_b32 %1, %3\n\t"
                 "v_max_f32 %0, %0, %2\n\tv_max_f32 %1, %1, %3\n\ts_nop 1\n\t"
                 "v_permlane16_swap_b32 %0, %1\n\t"
                 "v_max_f32 %0, %0, %1\n\t"
                 "v_mov_b32 %1, %0\n\ts_nop 1\n\t"
                 "v_permlane16_swap_b32 %0, %1\n\t"
                 "v_mov_b32 %2, %0\n\tv_mov_b32 %3, %1\n\ts_nop 0\n\t"
                 "v_permlane32_swap_b32 %0, %2\n\tv_permlane32_swap_b32 %1, %3"
                 : "=&v"(mx[0]), "=&v"(mx[1]), "=&v"(mx[2]), "=&v"(mx[3])
                 : "v"(sc[0][0]), "v"(sc[0][1]), "v"(sc[0][2]), "v"(sc[0][3]), "v"(sc[1][0]), "v"(sc[1][1]), "v"(sc[1][2]), "v"(sc[1][3]),
                   "v"(sc[2][0]), "v"(sc[2][1]), "v"(sc[2][2]), "v"(sc[2][3]), "v"(sc[3][0]), "v"(sc[3][1]), "v"(sc[3][2]), "v"(sc[3][3]));
    f32x2 e[8];
#pragma unroll
    for (int u = 0; u < 4; u++) mx[u] = -mx[u] * qs[1 + 4 * g2 + u];
    SB();
#pragma unroll
    for (int u = 0; u < 4; u++) {
      e[2 * u] = fma2(lo2(sc[u]), sp2(qs[1 + 4 * g2 + u]), sp2(mx[u])); e[2 * u + 1] = fma2(hi2(sc[u]), sp2(qs[1 + 4 * g2 + u]), sp2(mx[u]));
    }
    SB();
#pragma unroll
    for (int u = 0; u < 8; u++) { e[u][0] = __builtin_amdgcn_exp2f(e[u][0]); e[u][1] = __builtin_amdgcn_exp2f(e[u][1]); }
    SB();
#pragma unroll
    for (int u = 0; u < 4; u++) pk[u] = u32x2{pack2(e[2 * u]), pack2(e[2 * u + 1])};
    u32x2 pc[4], pp[4];
#pragma unroll
    for (int u = 0; u < 4; u++) { pc[u] = pk[u] & selc; pp[u] = pk[u] & ~selc; }
    SB();
#pragma unroll
    for (int u = 0; u < 4; u++) { sm[u] = mfma16(ones, pk[u], zero); o[u] = mfma16(V[1 + 4 * g2 + u], pc[u], zero); }
#pragma unroll
    for (int u = 0; u < 4; u++) { const int b = 1 + 4 * g2 + u, pb = (b <= 2) ? 0 : b - 2; o[u] = mfma16(V[pb], pp[u], o[u]); }
    SB();
    float inv[4];
#pragma unroll
    for (int u = 0; u < 4; u++) inv[u] = __builtin_amdgcn_rcpf(sm[u][0]);
    u32x2 O[4];
#pragma unroll
    for (int u = 0; u < 4; u++) O[u] = u32x2{pack2(lo2(o[u]) * sp2(inv[u])), pack2(hi2(o[u]) * sp2(inv[u]))};
    if (g2 == 2) lds_arrive(L_SYNC, (int)(ti - L_STAT + tq - L_STAT));
    if (g2 < 2 || P < 10) {
#pragma unroll
      for (int g = 0; g < 2; g++) {
        auto a = __builtin_amdgcn_permlane16_swap(O[2 * g][0], O[2 * g + 1][0], false, false);
        auto b2 = __builtin_amdgcn_permlane16_swap(O[2 * g][1], O[2 * g + 1][1], false, false);
        i32x4 v = {(int)a[0], (int)b2[0], (int)a[1], (int)b2[1]};
        store16_sc1(v, voff, sa, sbase + (4 * g2 + 2 * g) * RBS);
      }
    }
  }
  waitv<0>();
  if (__builtin_amdgcn_readfirstlane(lds_arrive(L_SYNC + 4, (int)(ti - L_STAT + tq - L_STAT))) == 3) flag_store(vfl, tag, sa);
}

DEV constexpr int zpage(int j, int l) {
  return j == 0 ? 28 + l : j == 1 ? 4 + l : j == 2 ? 8 + l : j == 3 ? 16 + l : j == 4 ? 0 + l
       : j == 5 ? (l < 2 ? 20 + l : 30 + l) : j == 6 ? 22 + l : j == 7 ? 34 + l : 12 + l;
}
DEV void lds_spin_ge(unsigned a, int c) {
  while (__builtin_amdgcn_readfirstlane(*(volatile LDS int*)(size_t)a) < c) __builtin_amdgcn_s_sleep(1);
  asm volatile("" ::: "memory");
}
DEV void lds_set(unsigned a, int lane) { if (lane == 0) *(volatile LDS unsigned*)(size_t)a = 1; }
DEV u64 poll_load(GLB u64* fl) { return __hip_atomic_load(fl, __ATOMIC_RELAXED, __HIP_MEMORY_SCOPE_AGENT); }
DEV void poll_wait(GLB u64* fl, u64 v, u64 tag) {
  if (!__all(v == tag)) {
    asm volatile("; slow path" ::: "memory");
    unsigned spins = 0;
    for (;;) {
      u64 t;
      __builtin_amdgcn_s_sleep(4);
      asm volatile("global_load_dwordx2 %0, %1, off sc1\n\ts_waitcnt vmcnt(0)" : "=&v"(t) : "v"(fl) : "memory");
      if (__all(t == tag) || ++spins > SPIN_CAP) break;
    }
  }
  __builtin_amdgcn_fence(__ATOMIC_ACQUIRE, "wavefront");
  asm volatile("" ::: "memory");
}
DEV void touch(const void* a) {
  asm volatile("s_mov_b32 m0, %0\n\ts_nop 0\n\tglobal_load_lds_dword %1, off" :: "s"(L_JUNK), "v"(a) : "memory");
}

enum { O_X, O_W, O_Z, O_H, O_RD, O_BAR, O_GLD, O_GCHK, O_TOUCH, O_WO, O_THR, O_HALO, O_PUB };
struct Op { int t, a, b; };
struct Prog {
  Op op[200]; int n;
  constexpr void add(int t, int a = 0, int b = 0) { op[n].t = t; op[n].a = a; op[n].b = b; n++; }
  constexpr Prog() : op{}, n(0) {
    add(O_X, 0, 0); add(O_W, 0); add(O_X, 0, 1); add(O_W, 1); add(O_X, 1, 0);
    int h = 0, rd = 0;
    add(O_BAR, -1);
    for (int s = -1; s < 16; s++) {
      const int c = (s + 1) >> 1;
      const bool doh = s >= 1;
      const int n0 = n;
      if (s == -1) add(O_X, 1, 1);
      else if (s & 1) { if (c + 1 < 8) add(O_X, c + 1, 1); if (s + 3 < 16) add(O_W, s + 3); }
      else { if (s == 0) add(O_W, 2); if (s + 3 < 16) add(O_W, s + 3); if (c + 2 < 8) add(O_X, c + 2, 0); }
      if (s == 12) add(O_Z, 0);
      if (s == 13) add(O_Z, 1);
      if (s == 14) { add(O_Z, 2); add(O_Z, 3); }
      if (s == 15) { add(O_Z, 4); add(O_Z, 5); }
      if (doh && h >= 2) {
        add(O_RD, rd++);
        for (int k = n - 1; k > n0 + 1; k--) { const Op t = op[k]; op[k] = op[k - 1]; op[k - 1] = t; }
      }
      if (doh) add(O_H, h++);
      if (s == -1) add(O_HALO, 0);
      if (s == 1) add(O_HALO, 1);
      if (s == 1) add(O_PUB);
      if (s == 0) add(O_TOUCH);
      if (s == 10) add(O_GLD);
      if (s == 13) add(O_GCHK);
      add(O_BAR, s + 1);
    }
    int z = 6; bool wo = false;
    while (rd < 18) {
      add(O_RD, rd++);
      if (!wo && after(n, O_Z, 4, 0) >= after(n, O_H, rd - 1, 0)) { add(O_WO); wo = true; }
      if (h < 18) add(O_H, h++);
      if (z < 8 && (rd & 1)) { add(O_THR); add(O_Z, z++); }
    }
    while (z < 8) add(O_Z, z++);
    add(O_Z, 8);
  }
  static constexpr int cnt(const Op& o) { return o.t == O_X ? 3 : (o.t == O_W || o.t == O_Z) ? 4 : o.t == O_H ? 2 : o.t == O_GLD ? 1 : 0; }
  constexpr int after(int pos, int t, int a, int b) const {
    int m = 0;
    for (int k = pos - 1; k >= 0; k--) { if (op[k].t == t && op[k].a == a && op[k].b == b) return m; m += cnt(op[k]); }
    return 63;
  }
};
constexpr Prog PROG{};
constexpr int cmin(int a, int b) { return a < b ? a : b; }

constexpr int DC = 6, DB = 6, DL = 8;

typedef int i32x16 __attribute__((ext_vector_type(16)));
DEV const char* kptr(const i32x16& k, int j) { return (const char*)(((u64)(unsigned)k[2 * j + 1] << 32) | (unsigned)k[2 * j]); }
#define KWAIT() Params p; { asm volatile("s_waitcnt lgkmcnt(0)" : "+s"(ka) :: "memory"); \
  p.x = kptr(ka, 0); p.wqkv = kptr(ka, 1); p.wo = kptr(ka, 2); p.wg = kptr(ka, 3); p.wu = kptr(ka, 4); p.wd = kptr(ka, 5); \
  p.out = (char*)kptr(ka, 6); p.ws = (char*)kptr(ka, 7); } \
  __amdgpu_buffer_rsrc_t ra = __builtin_amdgcn_make_buffer_rsrc(p.ws + WS_A, 0, (int)(WS_A_BYTES + 32 * FSTR), 0x00020000)

__global__ __launch_bounds__(512, 2) void fused_kernel(Params p_) {
  __shared__ __attribute__((aligned(16))) char smem[LDS_BYTES];
  asm volatile("" :: "s"((unsigned)(size_t)(LDS char*)smem));
  const int w = __builtin_amdgcn_readfirstlane(threadIdx.x >> 6);
#define DECODE() \
  int bid = blockIdx.x; asm volatile("" : "+s"(bid)); \
  const int xcd = bid & 7, r = bid >> 3, rh = xcd & 1; \
  const bool is_mlp = r < 21; \
  const unsigned idx = (xcd >> 1) * 21 + r, Sm = (idx * 43u) >> 8, pm = idx - 6 * Sm, hi3 = (pm + 1) >> 2;         \
  const unsigned jq = rh * 11 + (r - 21); \
  const int P = ssel(is_mlp, 6 * rh + pm, jq >> 1), S = ssel(is_mlp, Sm, (xcd & 6) + (jq & 1)); \
  const int P3 = 2 * rh + hi3, t3 = pm - 3 * hi3;         \
  const int rt2 = 4 * xcd + (r & 3)
#define RBASE ssel(is_mlp, 64 * rh + 11 * pm - hi3, 12 * P)
#define MTA (512 * P3 + (t3 == 0 ? 160 : t3 == 1 ? 321 : 337))
#define MTB (512 * P3 + (t3 == 1 ? 184 : 176))
#define CT2(wd) (((r >> 2) + 5 - (unsigned)((size_t)(wd) >> 7)) & 7)
#define ZKS(n) ((n) < 20 ? 52 + (n) : (n) - 20)
#define W2_OFFS() unsigned wb[4]; { const int krow = 4 * q + (i >> 2); \
    _Pragma("unroll") for (int cb = 0; cb < 4; cb++) wb[cb] = krow * 128 + ((cb ^ (krow >> 1)) & 3) * 32 + (i & 3) * 8; }

  asm volatile("s_cmp_gt_u32 %0, 3\n\ts_cbranch_scc1 .Lfk_skip\n\t"
               "s_load_dwordx2 s[8:9], %7, 0x0\n\t"
               "s_cmp_lg_u32 %0, 0\n\ts_cbranch_scc1 .Lfk_rows\n\ts_cmp_lt_u32 %1, %6\n\ts_cbranch_scc1 .Lfk_rows\n\t"
               "s_getpc_b64 s[4:5]\n.Lfk_code0:\n\t"
               "s_bfe_u32 s6, %1, %3\n\ts_mul_i32 s6, s6, %4\n\t"
               "v_min_u32 v1, %4 - 1, %2\n\tv_add_lshl_u32 v1, v1, s6, 7\n\t"
               "s_mov_b32 s6, .Lfk_code1-.Lfk_code0\n\ts_mov_b32 m0, %5\n\tv_min_u32 v1, s6, v1\n\t"
               "global_load_lds_dword v1, s[4:5]\n"
               ".Lfk_rows:\n\t"
               "s_and_b32 s4, %1, 1\n\ts_lshl_b32 s4, s4, 20\n\ts_bfe_u32 s5, %1, 0x20001\n\ts_lshl_b32 s5, s5, 18\n\ts_or_b32 s4, s4, s5\n\t"
               "s_and_b32 s5, %1, 0xf8\n\ts_lshl_b32 s5, s5, 10\n\ts_or_b32 s4, s4, s5\n\ts_lshl_b32 s5, %0, 10\n\ts_add_u32 s4, s4, s5\n\t"
               "v_lshl_add_u32 v1, %2, 4, s4\n\t"
               "s_lshl_b32 s5, %0, 12\n\ts_add_u32 m0, s5, 0xc000\n\ts_waitcnt lgkmcnt(0)\n\t"
               "global_load_lds_dwordx4 v1, s[8:9]\n\tglobal_load_lds_dwordx4 v1, s[8:9] offset:1024\n"
               ".Lfk_skip:"
               :: "s"(w), "s"(blockIdx.x), "v"(threadIdx.x), "n"(3 | (3 << 16)), "n"(48), "n"(L_JUNK), "n"(192),
                  "s"(__builtin_amdgcn_kernarg_segment_ptr())
               : "memory", "scc", "s4", "s5", "s6", "s8", "s9", "v1");
  if (w < 4) {
    DECODE();
    Params p = p_;
    int ct2 = CT2(p.wd);
    asm volatile("" : "+s"(p.x), "+s"(p.out), "+s"(p.ws), "+s"(ct2));
    GLB u64* flags = (GLB u64*)(p.ws + WS_FLAG);
    __amdgpu_buffer_rsrc_t ra = __builtin_amdgcn_make_buffer_rsrc(p.ws + WS_A, 0, (int)(WS_A_BYTES + 32 * FSTR), 0x00020000);
    if (is_mlp) mlp_path(p, w, P, P3, t3, S); else qkv_path(p, w, P, S);
    const int lane = fresh_lane(), i = lane & 15, q = lane >> 4;
    const u64 tag = *(const LDS u64*)(size_t)L_TAG;
    W2_OFFS();
    const int otok = tok_of(4 * rt2 + w, i);
    unsigned oadr = otok * 1024 + ct2 * 128 + (q & 1) * 32 + (q >> 1) * 16;
    asm volatile("" : "+v"(oadr));
    u32x2 xres[4];
#pragma unroll
    for (int cb = 0; cb < 4; cb++) xres[cb] = *(const GLB u32x2*)((const GLB char*)p.x + (unsigned)(otok * 1024 + ct2 * 128 + cb * 32 + q * 8));
    f32x4 as[4], ad[4], t[3][4]; f32x2 xv[8]; unsigned pa[4];
#pragma unroll
    for (int cb = 0; cb < 4; cb++) { as[cb] = f32x4{0.f, 0.f, 0.f, 0.f}; ad[cb] = f32x4{0.f, 0.f, 0.f, 0.f}; asm volatile("" : "+v"(as[cb]), "+v"(ad[cb])); }
    {
      const unsigned abase = (rt2 * 4 + w) * RBS;
      unsigned wbb[3][4];
#pragma unroll
      for (int g = 0; g < 3; g++)
#pragma unroll
        for (int cb = 0; cb < 4; cb++) { wbb[g][cb] = wb[cb] + g * 65536; asm volatile("" : "+v"(wbb[g][cb])); }
      i32x4 af[36]; s16x4 bh[36][8];
      auto ld = [&](int n) { af[n] = __builtin_amdgcn_raw_buffer_load_b128(ra, frag_off(lane), abase + ZKS(n) * FRG, 16); };
      auto rd = [&](int n, int h) {
        const int pg = zpage(n >> 2, n & 3);
        bh[n][h] = lds_tr4(wbb[pg >> 4][h >> 1] + (pg & 15) * 4096 + (h & 1) * 2048);
      };
      {
        const unsigned z0 = zpage(0, 0) * 4096, z1 = zpage(1, 0) * 4096, z2 = zpage(2, 0) * 4096, z3 = zpage(3, 0) * 4096;
#pragma unroll
        for (int a = 0; a < 4; a++) {
          const int l = (w - a - 1) & 3;
          pa[a] = (l == 0 ? z0 : l == 1 ? z1 : l == 2 ? z2 : z3) + a * 4096 + lane * 16; asm volatile("" : "+v"(pa[a]));
        }
      }
      GLB u64* flo = flags + rt2 * (FSTR / 8) + (28 + (lane & 7)) * 16;
      u64 po;
      lds_spin4(L_SYNC + 16);
#pragma unroll
      for (int n = 0; n < DB; n++)
#pragma unroll
        for (int h = 0; h < 8; h++) rd(n, h);
      lds_spin_ge(L_SYNC + 64, 1);
#pragma unroll
      for (int n = 0; n < DC; n++) ld(n);
      waitl();
      SB();
      sfor<0, 36>([&](auto nc) __attribute__((always_inline)) {
        constexpr int n = decltype(nc)::v;
        if (n + DB == 20) { waitl(); lds_set(L_SYNC + 48 + w * 4, lane); lds_spin4(L_SYNC + 32); }
        if (n == 34) {
          lds_spin4(L_SYNC + 80);
#pragma unroll
          for (int a = 0; a < 3; a++)
#pragma unroll
            for (int cb = 0; cb < 4; cb++) t[a][cb] = ldsf4(pa[a] + cb * 1024);
        }
        if (n == 31) {
#pragma unroll
          for (int j = 0; j < 8; j++) { xv[j] = f32x2{__uint_as_float(xres[j >> 1][j & 1] << 16), __uint_as_float(xres[j >> 1][j & 1] & 0xffff0000u)}; asm volatile("" : "+v"(xv[j])); }
        }
        if (n + 2 * DC == 20) po = poll_load(flo);
        if (n + DC == 20) poll_wait(flo, po, tag);
        if constexpr (n + DC < 36) ld(n + DC);
#pragma unroll
        for (int cb = 0; cb < 4; cb++) {
          mfma_acc(n < 20 ? ad[cb] : as[cb], cat(bh[n][2 * cb], bh[n][2 * cb + 1]), __builtin_bit_cast(s16x8, af[n]));
          if constexpr (n + DB < 36) { rd(n + DB, 2 * cb); rd(n + DB, 2 * cb + 1); }
          SB();
        }
      });
      asm volatile("s_nop 7" ::: "memory");
      SB();
    }
    {
#pragma unroll
      for (int cb = 0; cb < 4; cb++) { ad[cb] += (t[0][cb] + t[1][cb]) + t[2][cb]; asm volatile("" : "+v"(ad[cb])); }
      SB();
      bar();
#pragma unroll
      for (int cb = 0; cb < 4; cb++) ad[cb] += ldsf4(pa[3] + cb * 1024);
    }
    f32x2 d[8], e[8];
#pragma unroll
    for (int j = 0; j < 8; j++) { d[j] = (j & 1) ? hi2(ad[j >> 1]) : lo2(ad[j >> 1]); e[j] = d[j] * sp2(-LOG2E); }
    SB();
#pragma unroll
    for (int j = 0; j < 8; j++) {
      e[j][0] = __builtin_amdgcn_exp2f(e[j][0]); e[j][1] = __builtin_amdgcn_exp2f(e[j][1]);
      d[j] *= (j & 1) ? hi2(as[j >> 1]) : lo2(as[j >> 1]);
    }
    SB();
#pragma unroll
    for (int j = 0; j < 8; j++) e[j] += sp2(1.f);
    SB();
#pragma unroll
    for (int j = 0; j < 8; j++) { e[j][0] = __builtin_amdgcn_rcpf(e[j][0]); e[j][1] = __builtin_amdgcn_rcpf(e[j][1]); }
    SB();
#pragma unroll
    for (int j = 0; j < 8; j++) d[j] = fma2(d[j], e[j], xv[j]);
    SB();
    unsigned o[4][2];
#pragma unroll
    for (int j = 0; j < 8; j++) o[j >> 1][j & 1] = pack2(d[j]);
#pragma unroll
    for (int c2 = 0; c2 < 2; c2++) {
      auto a = __builtin_amdgcn_permlane16_swap(o[2 * c2][0], o[2 * c2 + 1][0], false, false);
      auto b = __builtin_amdgcn_permlane16_swap(o[2 * c2][1], o[2 * c2 + 1][1], false, false);
      u32x4 v = {a[0], b[0], a[1], b[1]};
      asm volatile("global_store_dwordx4 %0, %1, %2 sc1\n\ts_nop 1" :: "v"(oadr), "v"(v), "s"(p.out + c2 * 64) : "memory");
    }
    asm volatile(".Lfk_code1:");
  } else {
    i32x16 ka;
    asm volatile("s_load_dwordx16 %0, %1, 0x0" : "=s"(ka) : "s"(__builtin_amdgcn_kernarg_segment_ptr()));
    __builtin_amdgcn_s_setprio(3);
    SB();
    DECODE();
    const int l = w - 4, m1 = is_mlp ? 1 : 0;
    const int lane = fresh_lane(), i = lane & 15, q = lane >> 4;
    const int rot = 4 * ((P + S) & 1) + 2 * rh;
    const unsigned vpat = ((lane >> 3) * 2048 + (((lane & 7) ^ (lane >> 4)) & 7) * 16) ^ ((l & 1) << 6);
    unsigned vx[6], vw[2], vh[2];
    vx[0] = vpat + (unsigned)(tok_of(RBASE + (l >> 1), (l & 1) * 8) + 2) * 1024;
    vx[1] = vx[0] + 31744; vx[2] = vx[0] + 2 * 31744;
    unsigned lxb = L_X + l * 6144, sx0 = rot * 128;
    asm volatile("" : "+v"(vx[0]), "+v"(vx[1]), "+v"(vx[2]));
    asm volatile("" : "+s"(lxb), "+s"(sx0));
    SB();
    KWAIT();
    const i32x4 srd_x = make_srd(p.x - 2048, 2050 * 1024);
    const u64 mxb = (!is_mlp || l == 0) ? ~0ull : l == 1 ? 0xffffffffffffffull : 0xffull;
    auto issue_x = [&](int c, int h) {
      const unsigned base = lxb + (c % 3) * XSLOT + h * 3072, so = c == 0 ? sx0 : ((c + rot) & 7) * 128;
      if (h) dma_x3m(base, vx[3], vx[4], vx[5], srd_x, so, mxb); else dma_x3(base, vx[0], vx[1], vx[2], srd_x, so);
    };
    auto issue_halo = [&](int c) {
      dma16(L_HALO + c * 2048 + 128, vh[0], srd_x, ((c + rot) & 7) * 128);
      dma16m(L_HALO + c * 2048 + 1152, vh[1], srd_x, ((c + rot) & 7) * 128, 0xffffffffffffffull);
    };
    issue_x(0, 0);
    SB();
    {
      const unsigned vm = q * 3584 + ((((i >> 1) ^ q) & 7) * 32 ^ ((l & 1) << 7)) + (i & 1) * 16;
      const unsigned vq = (lane >> 3) * 1024 + (((lane & 7) * 16) ^ ((l & 1) << 6));
      vw[0] = (is_mlp ? vm : vq) + (l * (24576 + 4096 * m1) + (S << (7 + m1)));
      vw[1] = (vw[0] ^ (32 - 32 * m1)) + (5120 + 8192 * m1);
    }
    const unsigned wpitch32 = 98304 + 16384 * m1;
    const u64 mw = is_mlp ? ~0ull : 0xffffffffffffull;
    const i32x4 srd_w0 = make_srd(is_mlp ? p.wg : p.wqkv, wpitch32 * 16);
    const i32x4 srd_w1 = make_srd(is_mlp ? p.wu : p.wqkv + 4 * 3072, is_mlp ? 512 * 3584 : 508 * 3072);
    auto issue_w = [&](int s) { dma_w4(l * 2048 + (s % 3) * 16384, vw[0], vw[1], srd_w0, srd_w1, ((s + 2 * rot) & 15) * wpitch32, mw); };
    issue_w(0);
    SB();
    vx[3] = vx[0] + 98304; vx[4] = vx[1] + 98304;
    vx[5] = vsel(is_mlp, vpat + (unsigned)ssel(l == 0, MTA, MTB) * 1024, vx[2] + 98304);
    vh[0] = vh[1] = 0;
    if (!is_mlp) {
      const int rho = 1 + (lane >> 3);
      vh[0] = (unsigned)(max(192 * P - 16, 0) + 2 + 2 * (rho & 7) + (rho >> 3)) * 1024 + (((lane & 7) ^ (rho >> 1)) & 7) * 16;
      vh[1] = (vh[0] ^ 64) + 1024;
      if (l < 2) issue_halo(l);
    }
    issue_x(0, 1);
    unsigned ep0, ep1;
    asm volatile("s_load_dword %0, %2, 0x0\n\ts_load_dword %1, %2, 0x4" : "=&s"(ep0), "=&s"(ep1) : "s"(p.ws + WS_EPOCH) : "memory");
    __builtin_amdgcn_s_setprio(0);
    issue_w(1); issue_x(1, 0);
    SB();
    const i32x4 srd_o = make_srd(p.wo, 512 * 1024), srd_d = make_srd(p.wd, 1792 * 1024);
    unsigned vd[4], vo[4], hb[4], gadr, tadr; const char* tp;
    auto issue_z = [&](int j) {
      if (j < 5) dma_p4(zpage(j, l) * 4096, vd, srd_d, (ZKS(4 * j + l) - 16) * 32768);
      else dma_p4(zpage(j, l) * 4096, vo, srd_o, ZKS(4 * j + l) * 32768);
    };
    s16x4 wr[18][4];
    const unsigned stg = (12 + l) * 4096;
    auto hk = [&](int j) { const int jj = j + xcd; return 9 * l + (jj >= 9 ? jj - 9 : jj); };
    auto issue_h = [&](int n) {
      dma_p2(stg + (n & 1) * 2048, vd[0], vd[1], srd_d, (hk(n >> 1) * 32 + 16 * (n & 1)) * 1024);
    };
    u64 tag;
    u32x4 gr;
    unsigned padr = L_PAY + lane * 4, v16 = lane * 16;
    const int srow = 1024 * rh + 8 * ((xcd >> 1) * 32 + r);
    {
      const int lane = fresh_lane(), i = lane & 15, q = lane >> 4;
      const int cw = l & 1;
      const unsigned line = (unsigned)(ssel(cw, r, bid) * 36 + min(lane, 35)) << (3 * cw);
      tp = (line < 4096 ? p.wo : p.wd) + (line < 4096 ? line : line + 5120) * 128;
      tp += ((3 - (unsigned)((size_t)tp >> 7)) & (7 * cw)) * 128;
      asm volatile("" : "+v"(tp), "+v"(padr), "+v"(v16));
      W2_OFFS();
      const unsigned w2col = CT2(p.wd) * 128 + ((((lane & 7) >> 1) ^ (lane >> 4)) & 3) * 32 + (lane & 1) * 16;
#pragma unroll
      for (int o = 0; o < 4; o++) {
        vd[o] = ((lane >> 3) + 7 * o) * 1024 + w2col;
        vo[o] = (8 * (lane >> 5) + ((lane >> 3) & 3) + 16 * (o & 1) + 4 * (o >> 1) - o) * 1024 + w2col;
        hb[o] = stg + wb[o];
        asm volatile("" : "+v"(vd[o]), "+v"(vo[o]), "+v"(hb[o]));
      }
      const int gn = l * 52 + min(lane, 51);
      const int spA = ssel(is_mlp, MTA, max(192 * P - 16, 0)), spB = ssel(is_mlp, MTB, spA + 1), sp0 = 192 - 32 * m1;
      const int tn = min(tok_of(RBASE + (gn >> 4), gn & 15), 2047), tsp = ((gn & 8) ? spB : spA) + 2 * (gn & 7);
      gadr = (gn >= sp0 ? tsp : tn) * 16;
      tadr = L_STAT + vsel(is_mlp, gn * 16, (gn & ~15) * 8 + (gn & 15) * 4);
      asm volatile("" : "+v"(gadr), "+v"(tadr));
      SB();
    }
    sfor<5, PROG.n>([&](auto pc) __attribute__((always_inline)) {
      constexpr int pos = decltype(pc)::v;
      constexpr Op o = PROG.op[pos];
      if constexpr (o.t == O_X) issue_x(o.a, o.b);
      if constexpr (o.t == O_W) issue_w(o.a);
      if constexpr (o.t == O_HALO) { if (!is_mlp && (o.a == 1 || l >= 2)) issue_halo(l + 4 * o.a); }
      if constexpr (o.t == O_Z) { if (o.a == 8) waitl(); issue_z(o.a); }
      if constexpr (o.t == O_H) { waitl(); issue_h(o.a); }
      if constexpr (o.t == O_THR) waitv<2>();
      if constexpr (o.t == O_RD) {
        waitv<cmin(63, PROG.after(pos, O_H, o.a, 0))>();
#pragma unroll
        for (int cb = 0; cb < 4; cb++) wr[o.a][cb] = lds_tr4(hb[cb] + (o.a & 1) * 2048);
        SB();
      }
      if constexpr (o.t == O_BAR) {
        constexpr int s = o.a;
        constexpr int nw = PROG.after(pos, O_W, s + 1, 0), nx = (s & 1) ? PROG.after(pos, O_X, (s + 1) >> 1, 1) : 63;
        if (cmin(nw, nx) < 63) waitv<cmin(63, cmin(nw, nx))>();
        if (s == 16) waitl();
        bar();
      }
      if constexpr (o.t == O_WO) { waitv<PROG.after(pos, O_Z, 4, 0)>(); lds_set(L_SYNC + 16 + l * 4, lane); }
      if constexpr (o.t == O_TOUCH) { if (l >= 2) touch(tp); }
      if constexpr (o.t == O_PUB) {
        unsigned tlo, thi;
        asm volatile("s_waitcnt lgkmcnt(0)\n\ts_add_u32 %0, %2, 1\n\ts_add_u32 %1, %3, 1" : "=&s"(tlo), "=&s"(thi) : "s"(ep0), "s"(ep1) : "memory", "scc");
        tag = ((u64)thi << 32) | tlo;
        if (l == 0 && lane < 8) {
          const unsigned pay = *(const LDS unsigned*)(size_t)padr;
          const u32x4 g = {pay, tlo, pay, thi};
          asm volatile("global_store_dwordx4 %0, %1, %2 sc1\n\ts_nop 1" :: "v"(v16), "v"(g), "s"(p.ws + WS_GRAN + srow * 16) : "memory");
        }
        if (l == 1 && lane == 0) *(LDS u32x4*)(size_t)L_TAG = u32x4{tlo, thi, tlo, thi};
      }
      if constexpr (o.t == O_GLD) {
        if (l == 0 && lane == 0) __hip_atomic_store((GLB u64*)(p.ws + WS_DONE) + bid, tag, __ATOMIC_RELAXED, __HIP_MEMORY_SCOPE_AGENT);
      }
      if constexpr (o.t == O_GLD) asm volatile("global_load_dwordx4 %0, %1, %2 sc1" : "=v"(gr) : "v"(gadr), "s"(p.ws + WS_GRAN) : "memory");
      if constexpr (o.t == O_GCHK) {
        waitv<PROG.after(pos, O_GLD, 0, 0)>();
        asm volatile("" : "+v"(gr));
        unsigned spins = 0;
        while (!__all(gr[1] == (unsigned)tag && gr[3] == (unsigned)(tag >> 32))) {
          if (++spins > SPIN_CAP) break;
          __builtin_amdgcn_s_sleep(1);
          asm volatile("global_load_dwordx4 %0, %1, %2 sc1\n\ts_waitcnt vmcnt(0)" : "=&v"(gr) : "v"(gadr), "s"(p.ws + WS_GRAN) : "memory");
        }
        const float nm = __uint_as_float(gr[0] << 16), sd = __uint_as_float(gr[0] & 0xffff0000u), rs = __builtin_amdgcn_rcpf(sd);
        if (is_mlp) *(LDS f32x4*)(size_t)tadr = f32x4{nm, sd, -LOG2E * rs, 0.f};
        else { *(LDS float*)(size_t)tadr = nm; *(LDS float*)(size_t)(tadr + 64) = rs; }
      }
    });
    if (bid == 0 && l == 0) {
      GLB u64* done = (GLB u64*)(p.ws + WS_DONE);
      unsigned spins = 0;
      for (;;) {
        bool ok = true;
#pragma unroll
        for (int j = 0; j < 4; j++) ok &= __hip_atomic_load(done + j * 64 + lane, __ATOMIC_RELAXED, __HIP_MEMORY_SCOPE_AGENT) == tag;
        if (__all(ok) || ++spins > SPIN_CAP) break;
        __builtin_amdgcn_s_sleep(1);
      }
      if (lane == 0) __hip_atomic_store((GLB u64*)(p.ws + WS_EPOCH), tag, __ATOMIC_RELAXED, __HIP_MEMORY_SCOPE_AGENT);
    }
    f32x4 acc[4][4];
    {
      const unsigned abase = rt2 * 4 * RBS + 16 * FRG;
      const int u8 = rt2 & 7, par0 = (3 * (rt2 >> 3) + (u8 < 3 ? 0 : u8 < 6 ? 1 : 2)) & 1, par1 = (u8 == 2 || u8 == 5) ? par0 ^ 1 : par0;
      i32x4 af[36];
      auto ld = [&](int n) { af[n] = __builtin_amdgcn_raw_buffer_load_b128(ra, frag_off(lane), abase + ((n / 9 + l + 1) & 3) * RBS + hk(n % 9) * FRG, 16); };
      unsigned pb = (l == 0 ? zpage(0, 0) : l == 1 ? zpage(1, 0) : l == 2 ? zpage(2, 0) : zpage(3, 0)) * 4096 + lane * 16;
      asm volatile("" : "+v"(pb));
      auto pw = [&](int t) { *(LDS f32x4*)(size_t)(pb + t * 1024) = acc[t >> 2][t & 3]; };
      waitv<0>();
      lds_set(L_SYNC + 32 + l * 4, lane);
      if (l == 0) {
        const unsigned vpoll = (unsigned)WS_FLAG + rt2 * FSTR + (((lane & 16) ? par1 : par0) * 14 + min(lane & 15, 13)) * 128;
        lds_spin_ge(L_SYNC, is_mlp ? 4 : 1);
        if (!is_mlp) __builtin_amdgcn_s_sleep(8);
        __builtin_amdgcn_s_sleep(8);
        unsigned spins = 0;
        for (;;) {
          u64 t;
          asm volatile("global_load_dwordx2 %0, %1, %2 sc1\n\ts_waitcnt vmcnt(0)" : "=&v"(t) : "v"(vpoll), "s"(p.ws) : "memory");
          if (__all(t == tag) || ++spins > SPIN_CAP) break;
          __builtin_amdgcn_s_sleep(4);
        }
        lds_set(L_SYNC + 64, lane);
        __builtin_amdgcn_fence(__ATOMIC_ACQUIRE, "wavefront");
        asm volatile("" ::: "memory");
      } else { lds_spin_ge(L_SYNC + 64, 1); }
#pragma unroll
      for (int n = 0; n < DL; n++) ld(n);
      SB();
      sfor<0, 36>([&](auto nc) __attribute__((always_inline)) {
        constexpr int n = decltype(nc)::v, m = n + DL;
        if (n == 20) lds_spin4(L_SYNC + 48);
#pragma unroll
        for (int cb = 0; cb < 4; cb++) {
          mfma_st<n % 9 == 0>(acc[n / 9][cb], cat(wr[2 * (n % 9)][cb], wr[2 * (n % 9) + 1][cb]), __builtin_bit_cast(s16x8, af[n]));
          if (cb == 1 && n >= 20 && n < 28) pw(n - 20);
          if (n == 28) pw(8 + cb);
          if (n == 29 && cb == 3) { waitl(); lds_set(L_SYNC + 80 + l * 4, lane); }
        }
        if constexpr (m < 36) ld(m);
        SB();
      });
      asm volatile("s_nop 7" ::: "memory");
      SB();
#pragma unroll
      for (int t = 12; t < 16; t++) pw(t);
    }
    waitl(); bar();
    asm volatile("s_endpgm");
  }
}

extern "C" void kernel_launch(void* const* d_in, const int* in_sizes, int n_in, void* d_out, int out_size,
                              void* d_ws, size_t ws_size, hipStream_t stream) {
  Params p;
  p.x = (const char*)d_in[0]; p.wqkv = (const char*)d_in[1]; p.wo = (const char*)d_in[2];
  p.wg = (const char*)d_in[4]; p.wu = (const char*)d_in[5]; p.wd = (const char*)d_in[6];
  p.out = (char*)d_out; p.ws = (char*)(((size_t)d_ws + 1023) & ~(size_t)1023);
  fused_kernel<<<256, 512, 0, stream>>>(p);
}
```
